```python
import torch

def _softmax(x, axis=-1):
    return torch.softmax(x, dim=axis)
def _gelu(x):
    return torch.nn.functional.gelu(x, approximate="tanh")
def _silu(x):
    return torch.nn.functional.silu(x)
def _sigmoid(x):
    return torch.sigmoid(x)
def _layernorm(x, axis=-1, eps=1e-5):
    m = x.mean(dim=axis, keepdim=True)
    v = x.var(dim=axis, keepdim=True, unbiased=False)
    return (x - m) / torch.sqrt(v + eps)
def _scatter_add(tgt, idx, src, axis=0):
    return tgt.clone().index_add_(axis, idx, src)
def _segment_sum(src, seg, n):
    out = torch.zeros((n,) + src.shape[1:], dtype=src.dtype)
    return out.index_add_(0, seg, src)
def _pad(x, pw):
    flat = tuple(v for pair in reversed(pw) for v in pair)
    return torch.nn.functional.pad(x, flat)

kernel_name = 'recurrent_scan__ssm_conv_h3__dense_activation_gelu__prompt'

def setup_inputs():

    _w = (lambda a: (lambda t: t.to(torch.bfloat16) if t.is_floating_point() else t)(torch.as_tensor(a)))
    def _A_setup():
        B, S, D = 1, 2048, 640
        ks = 4
        return {
            "q": _w((lambda *s: torch.randn(*s, dtype=torch.bfloat16) * 0.05)(B, S, D)),
            "k": _w((lambda *s: torch.randn(*s, dtype=torch.bfloat16) * 0.8)(B, S, D)),
            "v": _w((lambda *s: torch.randn(*s, dtype=torch.bfloat16) * 0.8)(B, S, D)),
            "shift_k": _w((lambda *s: torch.randn(*s, dtype=torch.bfloat16) * 0.8)(D, ks) * 0.5),
            "ssm_k": _w((lambda *s: torch.randn(*s, dtype=torch.bfloat16) * 0.8)(D, S) * (1.0 / S ** 0.5)),
            "ssm_D": _w((lambda *s: torch.randn(*s, dtype=torch.bfloat16) * 0.8)(D)),
        }
    _A_in = _A_setup()

    def _B_setup():
        B, S, D, F = 1, 2048, 640, 1728
        return {
            "x": _w((lambda *s: torch.randn(*s, dtype=torch.bfloat16) * 0.05)(B, S, D)),
            "w1": _w((lambda *s: torch.randn(*s, dtype=torch.bfloat16) * 0.2)(D, F) * (D ** -0.5)),
            "b1": _w((lambda *s: torch.randn(*s, dtype=torch.bfloat16) * 0.2)(F) * 0.01),
            "w2": _w((lambda *s: torch.randn(*s, dtype=torch.bfloat16) * 0.2)(F, D) * (F ** -0.5)),
            "b2": _w((lambda *s: torch.randn(*s, dtype=torch.bfloat16) * 0.2)(D) * 0.01),
        }
    _B_in = _B_setup()

    return {
        "A_q": _A_in["q"],
        "A_k": _A_in["k"],
        "A_v": _A_in["v"],
        "A_shift_k": _A_in["shift_k"],
        "A_ssm_k": _A_in["ssm_k"],
        "A_ssm_D": _A_in["ssm_D"],
        "B_x": _B_in["x"],
        "B_w1": _B_in["w1"],
        "B_b1": _B_in["b1"],
        "B_w2": _B_in["w2"],
        "B_b2": _B_in["b2"],
    }

def reference(A_q, A_k, A_v, A_shift_k, A_ssm_k, A_ssm_D, B_x, B_w1, B_b1, B_w2, B_b2):
    def _A_ref(q, k, v, shift_k, ssm_k, ssm_D):
        B, S, D = q.shape
        ks = shift_k.shape[-1]
        k_pad = _pad(k, ((0, 0), (ks - 1, 0), (0, 0)))
        k_shift = torch.zeros_like(k)
        for t in range(ks):
            k_shift = k_shift + k_pad[:, t:t + S, :] * shift_k[:, ks - 1 - t][None, None, :]
        kv = k_shift * v
        L = 2 * S
        kv_pad = _pad(kv, ((0, 0), (0, S), (0, 0)))
        kf_pad = _pad(ssm_k, ((0, 0), (0, S)))
        kv_f = (lambda x, axes=(-2,-1): torch.fft.rfft2(x.float(), dim=axes))(kv_pad[:, None, :, :], axes=(1, 2))
        kf_f = (lambda x, axes=(-2,-1): torch.fft.rfft2(x.float(), dim=axes))(kf_pad[:, None, :], axes=(1, 2))
        kf_f = (lambda x, *d: x.permute(*d))(kf_f, 1, 2, 0)
        y_f = kv_f * kf_f[None, :, :, :]
        y = (lambda x, s, axes=(-2,-1): torch.fft.irfft2(x, s=s, dim=axes).to(torch.bfloat16))(y_f, (1, L), axes=(1, 2))[:, 0, :S, :]
        y = y + kv * ssm_D[None, None, :]
        return q * y

    def _B_ref(x, w1, b1, w2, b2):
        h = torch.einsum("bsd,df->bsf", x, w1) + b1
        h = _gelu(h)
        return torch.einsum("bsf,fd->bsd", h, w2) + b2

    x = A_q
    xn = _layernorm(x, axis=-1)
    s = _A_ref(xn, A_k, A_v, A_shift_k, A_ssm_k, A_ssm_D)
    g = _silu(_B_ref(xn, B_w1, B_b1, B_w2, B_b2))
    return x + s * g
```

```cpp
#include <hip/hip_runtime.h>
#include <stdint.h>

namespace h3 {
#define LAS __attribute__((address_space(3)))
#define GAS __attribute__((address_space(1)))
typedef unsigned v4u __attribute__((ext_vector_type(4)));
typedef unsigned v2u __attribute__((ext_vector_type(2)));
typedef float f32x4 __attribute__((ext_vector_type(4)));
typedef float f32x2 __attribute__((ext_vector_type(2)));
typedef float f32x16 __attribute__((ext_vector_type(16)));
typedef float f32x32 __attribute__((ext_vector_type(32)));
typedef unsigned u32x32 __attribute__((ext_vector_type(32)));
typedef int i32x16 __attribute__((ext_vector_type(16)));
typedef short s16x8 __attribute__((ext_vector_type(8)));
typedef short s16x4 __attribute__((ext_vector_type(4)));
typedef __bf16 bf2_t __attribute__((ext_vector_type(2)));
typedef unsigned long long u64;
typedef __amdgpu_buffer_rsrc_t rsrc_t;

constexpr int S = 2048, D = 640, F = 1728, KS2 = 54, NT = 6;
constexpr int SC1 = 16;
#define H_AUX SC1
#define KV_AUX 0
#define LAYOUT5
constexpr int WS_EPOCH = 0;
constexpr int WS_STAT = 4096;
constexpr int WS_CS = 20480;
constexpr int HS = 129 * 1024;
constexpr int WS_HT = 196608;
constexpr int WS_KVT = WS_HT + KS2 * HS;
constexpr int WS_YT = WS_KVT + D * S * 2;
constexpr int WS_FL = WS_YT + D * S * 2;
constexpr int FSTR = 128;
constexpr int FH = 0;
constexpr int FK = 1024;
constexpr int FY = 1664;
constexpr int WS_END = WS_FL + 2944 * FSTR;
__device__ __forceinline__ int flag_at(int n) { return WS_FL + n * FSTR; }
constexpr int WS_STAMP = 32 << 20;
constexpr int OOB = 0x40000000;
constexpr int L_W1 = 49152;
constexpr int L_KV = 98304, L_KT = L_KV + 18432;
constexpr int L_XS = 81920;
constexpr int L_OUT = 98304;
constexpr int C_A2 = 4128, C_B = 8256, CONV_BUF = 12864;
constexpr int L_CONV = 131072;
constexpr int L_STAT = L_CONV + 2 * CONV_BUF, L_CS = L_STAT + 1024, L_STAT2 = L_CS + 512, L_FLAG = L_STAT2 + 768, L_DUMP = L_FLAG + 256;
constexpr int LDS_BYTES = 163840;
static_assert(L_DUMP + 2048 <= LDS_BYTES, "lds");
constexpr int F_PAIR = 0, F_W2 = 8, F_STAT = 12, F_GELU = 16, F_RING = 20, F_STAGED = 24;
#define CODE_BYTES 32768
constexpr int SPIN_CAP = 1 << 15;

struct Params {
  const char *x, *k, *v, *sk, *hk, *dv, *w1, *b1, *w2, *b2;
  char *out, *ws;
};

__device__ __forceinline__ rsrc_t mk(const void* p, int bytes) { return __builtin_amdgcn_make_buffer_rsrc((void*)p, 0, bytes, 0x00020000); }
__device__ __forceinline__ f32x4 mfma(v4u a, v4u b, f32x4 c) {
  return __builtin_amdgcn_mfma_f32_16x16x32_bf16(__builtin_bit_cast(s16x8, a), __builtin_bit_cast(s16x8, b), c, 0, 0, 0);
}
__device__ __forceinline__ v2u trread(LAS char* a) { return __builtin_bit_cast(v2u, __builtin_amdgcn_ds_read_tr16_b64_v4i16((LAS s16x4*)a)); }
__device__ __forceinline__ v4u cat(v2u a, v2u b) { return v4u{a.x, a.y, b.x, b.y}; }
__device__ __forceinline__ float blo(unsigned u) { return __uint_as_float(u << 16); }
__device__ __forceinline__ float bhi(unsigned u) { return __uint_as_float(u & 0xffff0000u); }
__device__ __forceinline__ unsigned pk(float a, float b) {
  bf2_t r;
  r[0] = (__bf16)a;
  r[1] = (__bf16)b;
  return __builtin_bit_cast(unsigned, r);
}
__device__ __forceinline__ float rb(float a) { return blo(pk(a, a)); }
__device__ __forceinline__ unsigned pks(float a, float b) {
  asm("" : "+v"(a));
  asm("" : "+v"(b));
  return pk(a, b);
}
__device__ __forceinline__ v4u mkd(const void* p, int bytes) {
  const u64 a = (u64)p;
  return v4u{(unsigned)a, (unsigned)(a >> 32) & 0xffffu, (unsigned)bytes, 0x00020000u};
}
__device__ __forceinline__ void dma16(v4u rs, LAS char* dst, int voff, int soff) {
  asm volatile("s_mov_b32 m0, %0\n\ts_nop 0\n\tbuffer_load_dwordx4 %1, %2, %3 offen lds" ::"s"((unsigned)(size_t)dst), "v"(voff), "s"(rs), "s"(soff)
               : "memory");
}
__device__ __forceinline__ void dma16c(v4u rs, LAS char* dst, int voff, int soff) {
  asm volatile("s_mov_b32 m0, %0\n\ts_nop 0\n\tbuffer_load_dwordx4 %1, %2, %3 offen sc1 lds" ::"s"((unsigned)(size_t)dst), "v"(voff), "s"(rs), "s"(soff)
               : "memory");
}
__device__ __forceinline__ void dma4(v4u rs, LAS char* dst, const int (&vo)[4], int soff) {
  asm volatile(
      "s_mov_b32 m0, %0\n\ts_nop 0\n\tbuffer_load_dwordx4 %1, %5, %6 offen lds\n\tbuffer_load_dwordx4 %2, %5, %6 offen offset:1024 lds\n\t"
      "buffer_load_dwordx4 %3, %5, %6 offen offset:2048 lds\n\tbuffer_load_dwordx4 %4, %5, %6 offen offset:3072 lds" ::"s"((unsigned)(size_t)dst),
      "v"(vo[0]), "v"(vo[1]), "v"(vo[2]), "v"(vo[3]), "s"(rs), "s"(soff)
      : "memory");
}
template <int AUX>
__device__ __forceinline__ void st128(v4u v, rsrc_t rs, int voff) { __builtin_amdgcn_raw_buffer_store_b128(v, rs, voff, 0, AUX); }
#define WAIT_V(n) asm volatile("s_waitcnt vmcnt(" #n ")" ::: "memory")
#define WAIT_L0() asm volatile("s_waitcnt lgkmcnt(0)" ::: "memory")
#define BAR()                          \
  do {                                 \
    WAIT_L0();                         \
    __builtin_amdgcn_s_barrier();      \
    asm volatile("" ::: "memory");     \
  } while (0)
#define SB() __builtin_amdgcn_sched_barrier(0)
#define RAW(idx, v)
#define ST(idx)
#define TS(k)
#define DUMP()

__device__ __forceinline__ int lane_id() {
  int l;
  asm volatile("v_mbcnt_lo_u32_b32 %0, -1, 0\n\tv_mbcnt_hi_u32_b32 %0, -1, %0" : "=v"(l));
  return l;
}
template <int NAP = 0>
__device__ __forceinline__ void wait_flag(const Params& p, int off, u64 tag) {
  int cnt = SPIN_CAP;
  u64 f;
  asm volatile(
      "1:\n"
      "buffer_load_dwordx2 %0, %2, %3, 0 offen sc1\n"
      "s_sub_u32 %1, %1, 1\n"
      "s_cbranch_scc1 2f\n"
      "s_sleep %5\n"
      "s_waitcnt vmcnt(0)\n"
      "v_cmp_ne_u64 vcc, %4, %0\n"
      "s_cbranch_vccnz 1b\n"
      "2:\n"
      "s_waitcnt vmcnt(0)\n"
      : "=&v"(f), "+s"(cnt)
      : "v"(off), "s"(mkd(p.ws, WS_END)), "s"(tag), "n"(NAP)
      : "memory", "vcc", "scc");
}
__device__ __forceinline__ void set_flag(const Params& p, int off, u64 tag) {
  __builtin_amdgcn_raw_buffer_store_b64(v2u{(unsigned)tag, (unsigned)(tag >> 32)}, mk(p.ws, WS_END), off, 0, SC1);
}
__device__ __forceinline__ void lds_post(LAS char* L, int idx, int val) {
  asm volatile("" ::: "memory");
  *(volatile LAS int*)(L + L_FLAG + idx * 4) = val;
}
__device__ __forceinline__ void lds_post2(LAS char* L, int idx) {
  asm volatile("" ::: "memory");
  *(volatile LAS v2u*)(L + L_FLAG + idx * 4) = v2u{1u, 1u};
}
template <int NAP = 0>
__device__ __forceinline__ void lds_wait(LAS char* L, int idx, int val) {
  int cnt = 1 << 22, f;
  asm volatile(
      "1:\n"
      "ds_read_b32 %0, %2\n"
      "s_sub_u32 %1, %1, 1\n"
      "s_cbranch_scc1 2f\n"
      ".if %4\n s_sleep %4\n .endif\n"
      "s_waitcnt lgkmcnt(0)\n"
      "v_cmp_gt_i32 vcc, %3, %0\n"
      "s_cbranch_vccnz 1b\n"
      "2:\n"
      "s_waitcnt lgkmcnt(0)\n"
      : "=&v"(f), "+s"(cnt)
      : "v"(L + L_FLAG + idx * 4), "s"(val), "n"(NAP)
      : "memory", "vcc", "scc");
}
template <int NAP = 0>
__device__ __forceinline__ void lds_wait4(LAS char* L, int idx, int lane) { lds_wait<NAP>(L, idx + (lane & 3), 1); }
__device__ __forceinline__ u64 tag_of(u64 e) { return e * 6364136223846793005ull + 1442695040888963407ull; }
__device__ __forceinline__ u64 ask_epoch(const Params& p) {
  u64 e;
  asm volatile("s_load_dwordx2 %0, %1, 0x0" : "=s"(e) : "s"(p.ws + WS_EPOCH) : "memory");
  return e;
}
__device__ __forceinline__ u64 get_tag(const Params& p) {
  u64 e;
  asm volatile("s_load_dwordx2 %0, %1, 0x0\n\ts_waitcnt lgkmcnt(0)" : "=s"(e) : "s"(p.ws + WS_EPOCH) : "memory");
  return tag_of(e);
}
__device__ __forceinline__ void code_prefetch(u64 pc, LAS char* dump, int r, int lane) {
  dma16(mkd((const void*)pc, CODE_BYTES), dump, r * 1024 + lane * 16, 0);
}

constexpr int KV_IN = 18432;
struct KvPre {
  v4u m, sf;
  int sel;
  LAS char *ka[2], *va, *sa, *ta[2];
};
__device__ __forceinline__ void kv_pre(KvPre& k, LAS char* Li, LAS char* Lt, int cb, int lane) {
  const int c = lane & 15, g = lane >> 4, tok = 4 * (c & 3) + (c >> 2);
#pragma unroll
  for (int d = 0; d < 4; ++d) k.m[d] = (g & 1) == (c >> 3) && d == ((c >> 1) & 3) ? (c & 1 ? 0xffff0000u : 0xffffu) : 0u;
  k.sf = k.m & ((g >> 1) == (cb & 1) ? 0x3f803f80u : 0u);
  k.sel = g & 2 ? 0x03020302 : 0x01000100;
#pragma unroll
  for (int y = 0; y < 2; ++y) {
    const int row = 8 + tok - 2 * y - (g >> 1);
    k.ka[y] = Li + row * 128 + (((2 * cb + (g & 1)) ^ (row & 7)) << 4);
  }
  k.va = Li + 9216 + tok * 128 + (((4 * (cb >> 1) + g) ^ (tok & 7)) << 4);
  k.sa = Li + 17408 + cb * 128 + c * 8;
  k.ta[0] = Lt + (16 * cb + c) * 128 + ((g ^ (c & 7)) << 4);
  k.ta[1] = Lt + (16 * cb + c) * 128 + (((4 + g) ^ (c & 7)) << 4);
  asm volatile("" : "+v"(k.m), "+v"(k.sf), "+v"(k.sel), "+v"(k.ka[0]), "+v"(k.ka[1]), "+v"(k.va), "+v"(k.sa), "+v"(k.ta[0]), "+v"(k.ta[1]));
}
__device__ __forceinline__ void kv_make(const KvPre& k, int n) {
  const v2u sk = *(LAS v2u*)(k.sa + n * KV_IN);
  v4u kf[4][2], vf[4];
#pragma unroll
  for (int q = 0; q < 4; ++q) {
    vf[q] = *(LAS v4u*)(k.va + n * KV_IN + q * 2048);
#pragma unroll
    for (int y = 0; y < 2; ++y) kf[q][y] = *(LAS v4u*)(k.ka[y] + n * KV_IN + q * 2048);
  }
  SB();
  const v4u w0 = k.m & __builtin_amdgcn_perm(sk.x, sk.x, k.sel), w1 = k.m & __builtin_amdgcn_perm(sk.y, sk.y, k.sel);
  const f32x4 Z = {0.f, 0.f, 0.f, 0.f};
  v2u o2[4];
#pragma unroll
  for (int q = 0; q < 4; ++q) {
    const f32x4 o = mfma(kf[q][1], w1, mfma(kf[q][0], w0, Z)) * mfma(vf[q], k.sf, Z);
    o2[q] = v2u{pk(o[0], o[1]), pk(o[2], o[3])};
  }
  *(LAS v4u*)(k.ta[0] + n * 8192) = cat(o2[0], o2[1]);
  *(LAS v4u*)(k.ta[1] + n * 8192) = cat(o2[2], o2[3]);
}
template <int N>
__device__ __forceinline__ void kv_store(const Params& p, LAS char* Lt, int n0, int pp, int G, int lane) {
  const int row = lane >> 3;
#pragma unroll
  for (int n = 0; n < N; ++n)
    st128<SC1>(*(LAS v4u*)(Lt + (n0 + n) * 1024 + lane * 16), mk(p.ws, WS_END),
               WS_KVT + (64 * G + 8 * (n0 + n) + row) * 4096 + 128 * pp + (((lane & 7) ^ row) << 4));
}

__device__ __forceinline__ int g2_step(int w, int rot, int k) {
  const int t = k + rot;
  return k == 13 ? 26 + 27 * (w >> 1) : 27 * (w >> 1) + 13 * (w & 1) + (t >= 13 ? t - 13 : t);
}
__device__ __forceinline__ int w2_block(int k) { return (0x53024176u >> (4 * (k & 7))) & 7; }
__device__ __forceinline__ void w2_src(int (&so)[4], int lane) {
  const int row = lane >> 3;
#pragma unroll
  for (int h = 0; h < 4; ++h) so[h] = (8 * h + row) * 1280 - 1024 * h + (((lane & 7) ^ (((row >> 1) & 3) << 1)) << 4);
}
__device__ __forceinline__ void w2_fetch(v4u rw2, LAS char* L, const int (&so)[4], int w, int rot, int k, int G2) {
  dma4(rw2, L + w2_block(k) * 16384 + w * 4096, so, g2_step(w, rot, k) * (32 * 1280) + G2 * 128);
}

__device__ __forceinline__ void stage_pads(LAS char* Lc, int lane) {
  if (lane < 16) *(LAS unsigned*)(Lc + (lane >> 3) * C_A2 + (lane & 7) * 4) = 0u;
  *(LAS v2u*)(Lc + C_B + (lane >> 4) * 1152 + (lane & 15) * 8) = v2u{0u, 0u};
}
template <int DELAYED>
__device__ __forceinline__ void stage_h(const Params& p, LAS char* Lc, int o) {
  const int vo[4] = {DELAYED ? max(o, 4) : o, o, o, o};
  dma4(mkd(p.hk - 4 * DELAYED, D * S * 2 + 4 * DELAYED), Lc + DELAYED * C_A2 + 32, vo, 0);
}
__device__ __forceinline__ void stage_u(const Params& p, LAS char* Lc, int ch, int lane) {
#pragma unroll
  for (int ph = 0; ph < 4; ++ph) dma16c(mkd(p.ws, WS_END), Lc + C_B + 128 + ph * 1152, lane * 64, WS_KVT + ch * 4096 + ph * 16);
}
__device__ __forceinline__ unsigned ld_skip(const Params& p, int ch) {
  unsigned dbits;
  asm volatile("s_load_dword %0, %1, 0x0\n\ts_waitcnt lgkmcnt(0)" : "=s"(dbits) : "s"(p.dv + (ch & ~1) * 2) : "memory");
  return ch & 1 ? dbits & 0xffff0000u : dbits << 16;
}
__device__ __forceinline__ void stage_patch(LAS char* Lc, unsigned dbits) {
  const v4u h = *(volatile LAS v4u*)(Lc + 32);
  const unsigned h01 = pk(blo(h.x) + __uint_as_float(dbits), bhi(h.x));
  *(volatile LAS unsigned*)(Lc + 32) = h01;
  *(volatile LAS v4u*)(Lc + C_A2 + 32) = v4u{0u, h01, h.y, h.z};
}

#define L_PRE 1
#define RI 1
#define RJ 1
__device__ __forceinline__ int rot_of(int i, int j) { return (RI * i + RJ * j) % 10; }
__device__ __forceinline__ u64 gemm1_load(const Params& p, LAS char* L, int i, int j, bool job, int G2, int rot2, int jb, int lw, int lane) {
  {
    const int row = lane >> 3, o = (64 * (jb & 31) + row) * 1280 + (jb >> 5) * 128 + (((lane & 7) ^ row) << 4);
    const int vo[4] = {o, o + 9216, o + 2 * 9216, o + 3 * 9216};
    const v4u rk = mkd(lw ? p.v : p.k, S * D * 2);
    LAS char* d = L + L_KV + 1024 + lw * 8192;
    dma4(rk, d, vo, 0);
    dma4(rk, d + 4096, vo, 32 * 1280);
    if (lw) dma16(mkd(p.sk, D * 8), L + L_KV + 17408, lane < 32 ? (jb >> 5) * 512 + lane * 16 : OOB, 0);
    else dma16(rk, L + L_KV, row >= 5 ? o - 8 * 1280 : OOB, 0);
  }
  TS(0);
  const v4u rx = mkd(p.x, S * D * 2), rw = mkd(p.w1, D * F * 2);
  const int i16 = lane & 15, g = lane >> 4;
  int xo[4], wo[4];
#pragma unroll
  for (int e = 0; e < 4; ++e) {
    xo[e] = (128 * i + 64 * lw + 8 * e + (lane >> 3)) * 1280 - 1024 * e + (((lane & 7) ^ (4 * (e & 1) + g)) << 4);
    const int c = i16 ^ ((g | ((e >> 1) << 2)) << 1);
    wo[e] = (32 * lw + 4 * e + g) * 3456 - 1024 * e + 256 * j + (c << 4);
    if (j == 13 && c >= 8) wo[e] = OOB;
  }
  const v4u rw2 = mkd(p.w2, job ? F * D * 2 : 0);
  int so[4];
  w2_src(so, lane);
  asm volatile("" : "+v"(so[0]), "+v"(so[1]), "+v"(so[2]), "+v"(so[3]));
  u64 ep;
  unsigned dw;
  {
    const int kst = rot_of(i, j), mb = (int)(size_t)L + lw * 8192;
    int sx = kst * 128, sw = kst * (64 * 3456), sm = mb, cnt = 9, t0, t1;
#define LD4(a, b, c, d, r, s)                                                                                            \
  "buffer_load_dwordx4 %" #a ", %" #r ", %" #s " offen lds\n buffer_load_dwordx4 %" #b ", %" #r ", %" #s " offen offset:1024 lds\n" \
  "buffer_load_dwordx4 %" #c ", %" #r ", %" #s " offen offset:2048 lds\n buffer_load_dwordx4 %" #d ", %" #r ", %" #s " offen offset:3072 lds\n"
    asm volatile(
        "s_load_dwordx2 %6, %19, 0x0\n"
        "s_mov_b32 m0, %2\n"
        "s_branch 2f\n"
        "1:\n"
        "s_waitcnt vmcnt(16) lgkmcnt(0)\n"
        "s_barrier\n"
        "2:\n" LD4(8, 9, 10, 11, 16, 0)
        "s_add_u32 m0, %2, 49152\n"
        "s_add_u32 %4, %0, 32*1280\n" LD4(12, 13, 14, 15, 17, 1)
        "s_add_u32 m0, %2, 4096\n"
        "s_add_u32 %5, %1, 16*3456\n" LD4(8, 9, 10, 11, 16, 4)
        "s_add_u32 m0, %2, 49152+4096\n"
        "s_add_u32 %0, %0, 128\n" LD4(12, 13, 14, 15, 17, 5)
        "s_add_u32 %1, %1, 64*3456\n"
        "s_cmp_eq_u32 %0, 1280\n"
        "s_cselect_b32 %0, 0, %0\n"
        "s_cselect_b32 %1, 0, %1\n"
        "s_add_u32 %2, %2, 16384\n"
        "s_sub_u32 %4, %2, %18\n"
        "s_cmp_eq_u32 %4, 49152\n"
        "s_cselect_b32 %2, %18, %2\n"
        "s_mov_b32 m0, %2\n"
        "s_sub_u32 %3, %3, 1\n"
        "s_cbranch_scc0 1b\n"
        "s_waitcnt vmcnt(16)\n"
        "s_barrier\n"
        : "+&s"(sx), "+&s"(sw), "+&s"(sm), "+&s"(cnt), "=&s"(t0), "=&s"(t1), "=&s"(ep), "=&s"(dw)
        : "v"(xo[0]), "v"(xo[1]), "v"(xo[2]), "v"(xo[3]), "v"(wo[0]), "v"(wo[1]), "v"(wo[2]), "v"(wo[3]), "s"(rx), "s"(rw), "s"(mb),
          "s"(p.ws + WS_EPOCH)
        : "memory", "scc");
  }
  TS(4);
  __builtin_amdgcn_s_setprio(0);
#pragma unroll
  for (int k = 0; k < 7; ++k) {
    if (k == 5) {
      __builtin_amdgcn_s_setprio(3);
      lds_wait4(L, F_RING, lane);
      __builtin_amdgcn_s_setprio(0);
    }
    w2_fetch(rw2, L, so, 2 * lw, rot2, k, G2);
    w2_fetch(rw2, L, so, 2 * lw + 1, rot2, k, G2);
    if (k == L_PRE - 1) {
      asm volatile("s_waitcnt vmcnt(%0)\n\ts_barrier" ::"n"(8 * L_PRE) : "memory");
      TS(5);
    }
    if (k == 2) TS(1);
    if (k == 4) TS(2);
  }
  TS(6);
  __builtin_amdgcn_s_setprio(3);
  WAIT_V(0);
  lds_post2(L, F_W2 + 2 * lw);
  TS(7);
  return tag_of(ep);
}

__device__ __forceinline__ u64 gemm1_side(const Params& p, LAS char* L, int i, int j, int t0, int G2, int jb, int sw, int lane) {
  LAS char* Lc = L + L_CONV + sw * CONV_BUF;
  const int ch = 2 * jb + sw;
  if (sw) *(LAS int*)(L + L_FLAG + lane * 4) = 0;
  stage_pads(Lc, lane);
  const u64 tag = get_tag(p);
  const unsigned t32 = (unsigned)(tag >> 32);
  const int g = lane >> 4, q = (lane >> 2) & 3;
  BAR();
  TS(0);
  BAR();
  TS(1);
  if (i < 10) {
    v4u sa[4][2];
#pragma unroll
    for (int u = 0; u < 4; ++u)
#pragma unroll
      for (int ks = 0; ks < 2; ++ks) {
        LAS char* aa = L + L_W1 + (32 * ks + 8 * g + q) * 256 + (((4 * sw + u) ^ (q | ((g & 1) << 2))) << 5) + 8 * (lane & 3);
        sa[u][ks] = cat(trread(aa), trread(aa + 1024));
      }
    f32x4 cs = {0.f, 0.f, 0.f, 0.f};
#pragma unroll
    for (int u = 0; u < 4; ++u) {
      const unsigned one = q == u ? 0x3f803f80u : 0u;
#pragma unroll
      for (int ks = 0; ks < 2; ++ks) cs = mfma(v4u{one, one, one, one}, sa[u][ks], cs);
    }
    __builtin_amdgcn_raw_buffer_store_b64(v2u{__float_as_uint(cs[0]), t32}, mk(p.ws, WS_END),
                                          WS_CS + (rot_of(i, j) * 1792 + 128 * j + 64 * sw + lane) * 8, 0, SC1);
  }
  BAR();
  TS(2);
  kv_store<4>(p, L + L_KT, 4 * sw, jb & 31, jb >> 5, lane);
  BAR();
  BAR();
  BAR();
  TS(3);
  WAIT_V(0);
  set_flag(p, lane == 0 ? flag_at(FK + (2 * (jb >> 5) + sw) * 32 + (jb & 31)) : OOB, tag);
  const unsigned dbits = ld_skip(p, ch);
  const v4u rwd = mkd(p.ws, WS_END);
  int o0 = WS_STAT + (128 * i + 64 * sw + lane) * 8, o1 = WS_CS + ((lane >> 5) * 1792 + 128 * j + 64 * sw + 2 * (lane & 31)) * 8;
  int o2 = WS_STAT + (16 * t0 + 48 * sw + min(lane, 47)) * 8, o3 = flag_at(FK + (ch >> 5) * 32 + (lane & 31));
  int xo = (16 * t0 + 48 * sw + (lane >> 3)) * 1280 + G2 * 128 + (((lane & 7) ^ (lane >> 3)) << 4);
  int ho = ch * 4096 + lane * 16;
  LAS char *d0 = L + L_STAT + (64 * sw + lane) * 8, *d1 = L + L_CS + (64 * sw + 2 * (lane & 31) + (lane >> 5)) * 4;
  LAS char* d2 = L + L_STAT2 + (48 * sw + min(lane, 47)) * 8;
  asm volatile("" : "+v"(o0), "+v"(o1), "+v"(o2), "+v"(o3), "+v"(xo), "+v"(ho), "+v"(d0), "+v"(d1), "+v"(d2));
  v2u g0, g2;
  v4u g1[5];
  u64 fk;
#define S_FETCH                                                                                                        \
  "buffer_load_dwordx2 %0, %8, %12, 0 offen sc1\n buffer_load_dwordx4 %1, %9, %12, 0 offen sc1\n"                      \
  "buffer_load_dwordx4 %2, %9, %12, %13 offen sc1\n buffer_load_dwordx4 %3, %9, %12, %14 offen sc1\n"                  \
  "buffer_load_dwordx4 %4, %9, %12, %15 offen sc1\n buffer_load_dwordx4 %5, %9, %12, %16 offen sc1\n"                  \
  "buffer_load_dwordx2 %6, %10, %12, 0 offen sc1\n buffer_load_dwordx2 %7, %11, %12, 0 offen sc1\n"
#define S_OPS                                                                                                          \
  : "=&v"(g0), "=&v"(g1[0]), "=&v"(g1[1]), "=&v"(g1[2]), "=&v"(g1[3]), "=&v"(g1[4]), "=&v"(g2), "=&v"(fk)              \
  : "v"(o0), "v"(o1), "v"(o2), "v"(o3), "s"(rwd), "s"(28672), "s"(2 * 28672), "s"(3 * 28672), "s"(4 * 28672)           \
  : "memory"
  asm volatile("s_barrier\n s_barrier\n s_barrier\n s_barrier\n" S_FETCH S_OPS);
  stage_h<0>(p, Lc, ho);
  __builtin_amdgcn_s_barrier();
  TS(4);
  asm volatile("s_waitcnt vmcnt(5)" : "+v"(g0), "+v"(g1[0]), "+v"(g1[1]), "+v"(g1[2]), "+v"(g1[3]), "+v"(g1[4]), "+v"(g2)::"memory");
  auto stale = [&]() {
    unsigned a = g0.y & g2.y, o = g0.y | g2.y;
#pragma unroll
    for (int n = 0; n < 5; ++n) {
      a &= g1[n].y & g1[n].w;
      o |= g1[n].y | g1[n].w;
    }
    return __builtin_amdgcn_ballot_w64(a != t32 || o != t32) != 0;
  };
  if (__builtin_expect(stale(), 0)) {
    int spin = SPIN_CAP;
#pragma unroll 1
    do asm volatile("s_waitcnt vmcnt(0)\n" S_FETCH "s_waitcnt vmcnt(0)" S_OPS);
    while (stale() && --spin);
  }
  {
    float ca = __uint_as_float(g1[0].x), cb = __uint_as_float(g1[0].z);
#pragma unroll
    for (int n = 1; n < 5; ++n) {
      ca += __uint_as_float(g1[n].x);
      cb += __uint_as_float(g1[n].z);
      asm("" : "+v"(ca));
    }
    const v2u sw2 = __builtin_amdgcn_permlane32_swap(__float_as_uint(ca), __float_as_uint(cb), false, false);
    *(LAS f32x2*)d0 = f32x2{blo(g0.x), __builtin_amdgcn_rcpf(bhi(g0.x))};
    *(LAS float*)d1 = __uint_as_float(sw2.x) + __uint_as_float(sw2.y);
  }
  lds_post2(L, F_STAT + 2 * sw);
  TS(5);
  asm volatile("s_waitcnt vmcnt(4)" : "+v"(fk)::"memory");
  if (__builtin_expect(__builtin_amdgcn_ballot_w64(fk != tag) != 0, 0)) wait_flag<2>(p, o3, tag);
  stage_u(p, Lc, ch, lane);
  stage_h<1>(p, Lc, ho);
  {
    const v4u rx = mkd(p.x, S * D * 2);
#pragma unroll
    for (int h = 0; h < 6; ++h) dma16(rx, L + L_XS + (6 * sw + h) * 1024, xo, h * 8 * 1280);
  }
  TS(6);
  *(LAS f32x2*)d2 = f32x2{blo(g2.x), __builtin_amdgcn_rcpf(bhi(g2.x))};
  WAIT_V(6);
  stage_patch(Lc, dbits);
  WAIT_V(0);
  lds_post2(L, F_STAGED + 2 * sw);
  TS(7);
  return tag;
}

template <int H>
__device__ __forceinline__ void conv_send(LAS char* Lc, const f32x32& z0, int lane) {
#pragma unroll
  for (int c = 4 * H; c < 4 * H + 4; ++c) *(LAS f32x4*)(Lc + c * 1024 + lane * 16) = f32x4{z0[4 * c], z0[4 * c + 1], z0[4 * c + 2], z0[4 * c + 3]};
  asm volatile("; half %0" ::"n"(H) : "memory");
}
template <int H>
__device__ __forceinline__ void conv_keep(LAS char* Lc, const f32x32& z0, int lane) {
  f32x4 y[4];
  const f32x4 c0 = *(LAS f32x4*)(Lc + C_B + H * 1024 + lane * 16), c1 = *(LAS f32x4*)(Lc + C_B + 2304 + H * 1024 + lane * 16);
#pragma unroll
  for (int c = 0; c < 4; ++c) y[c] = *(LAS f32x4*)(Lc + (4 * H + c) * 1024 + lane * 16);
  SB();
#pragma unroll
  for (int c = 0; c < 4; ++c) {
    const int b = 16 * H + 4 * c;
    *(LAS v2u*)(Lc + H * 4096 + 512 * c + 32 * (lane & 15) + 8 * (lane >> 4)) =
        v2u{pks(y[c][0] + z0[b] + c0[c] + c1[c], y[c][1] + z0[b + 1]), pks(y[c][2] + z0[b + 2], y[c][3] + z0[b + 3])};
  }
  asm volatile("; half %0" ::"n"(H) : "memory");
}
__device__ __forceinline__ void publish_y(const Params& p, int ch, int pw, u64 tag, int lane) {
  WAIT_V(0);
  set_flag(p, lane == 0 ? flag_at(FY + 2 * ch + pw) : OOB, tag);
}
__device__ __forceinline__ void conv(const Params& p, LAS char* L, LAS char* Lc, int ch, int me, int round, int gate, u64 tag, int lane) {
  const int pw = me & 1, col = lane & 15, g = lane >> 4;
  const int aa = (int)(size_t)Lc + pw * C_A2 + 32 + 64 * g - 8 * ((lane >> 2) & 3) + 8 * (lane & 3);
  const int ba = (int)(size_t)Lc + C_B + pw * 2304 + 56 + 8 * col - 16 * g;
  u32x32 a0, a1, b0, b1;
  asm volatile(
      ".set n, 0\n"
      ".rept 16\n"
      " ds_read_b64_tr_b16 v[4*n:4*n+1], v192 offset:256*n+32\n"
      " ds_read_b64_tr_b16 v[4*n+2:4*n+3], v192 offset:256*n\n"
      " ds_read_b64 v[64+4*n:65+4*n], v193 offset:1152+64*n\n"
      " ds_read_b64 v[66+4*n:67+4*n], v193 offset:1160+64*n\n"
      " .set n, n+1\n"
      ".endr\n"
      "s_waitcnt lgkmcnt(0)\n"
      : "=&{v[0:31]}"(a0), "=&{v[32:63]}"(a1), "=&{v[64:95]}"(b0), "=&{v[96:127]}"(b1)
      : "{v192}"(aa), "{v193}"(ba)
      : "memory");
  lds_post(L, F_PAIR + me, round ? 7 : 1);
  ST(8);
  if (round) publish_y(p, ch - 4, pw, tag, lane);
  if (gate >= 0) lds_wait<1>(L, gate, 1);
  ST(9);
  f32x32 z1, z0;
  asm volatile(
      "s_setprio 1\n"
      ".set m, 0\n"
      ".set r, 0\n"
      ".set y, 0\n"
      ".rept 2\n"
      " .set c, 7\n"
      " .rept 8\n"
      "  .set n, 0\n"
      "  .rept 2*c+2\n"
      "   .if m == 72\n"
      "    s_waitcnt lgkmcnt(4)\n"
      "   .endif\n"
      "   .if m == 84\n"
      "    s_waitcnt lgkmcnt(0)\n"
      "   .endif\n"
      "   .set z, 128+32*(m/72)+4*c\n"
      "   .set b, 64+4*(2*c+1-n)\n"
      "   .if (m < 72) && (n == 0)\n"
      "    v_mfma_f32_16x16x32_bf16 v[z:z+3], v[0:3], v[b:b+3], 0\n"
      "   .else\n"
      "    v_mfma_f32_16x16x32_bf16 v[z:z+3], v[4*n:4*n+3], v[b:b+3], v[z:z+3]\n"
      "   .endif\n"
      "   .set cr, 7-r/4\n"
      "   .set cy, 7-y/4\n"
      "   .if (r < 32) && ((m >= 72) || (c < cr) || ((c == cr) && (n >= 2)))\n"
      "    .set b, 2*cr+1-(r&3)/2\n"
      "    ds_read_b64 v[64+4*b+2*(r&1):65+4*b+2*(r&1)], v193 offset:64*b+8*(r&1)\n"
      "    .set r, r+1\n"
      "   .elseif (y < 32) && (m >= 80-cy*(cy+1))\n"
      "    .if y&3\n"
      "     v_mov_b32 v[160+4*cy+(y&3)], v[127+4*cy+(y&3)]\n"
      "    .else\n"
      "     v_mov_b32 v[160+4*cy], 0\n"
      "    .endif\n"
      "    .set y, y+1\n"
      "   .endif\n"
      "   .set m, m+1\n"
      "   .set n, n+1\n"
      "  .endr\n"
      "  .set c, c-1\n"
      " .endr\n"
      ".endr\n"
      "s_setprio 3\n"
      "s_nop 6\n"
      : "=&{v[128:159]}"(z1), "=&{v[160:191]}"(z0), "+{v[64:95]}"(b0), "+{v[96:127]}"(b1)
      : "{v[0:31]}"(a0), "{v[32:63]}"(a1), "{v193}"(ba)
      : "memory");
  ST(10);
  {
    const int Q = (4 * col + g + 1) & 63;
    LAS char* d = Lc + C_B + pw * 2304 + (Q & 3) * 256 + (Q >> 2) * 16;
    const bool last = lane == 63;
    *(LAS f32x4*)d = f32x4{last ? 0.f : z1[3], last ? z1[3] : z1[7], last ? z1[7] : z1[11], last ? z1[11] : z1[15]};
    *(LAS f32x4*)(d + 1024) = f32x4{last ? z1[15] : z1[19], last ? z1[19] : z1[23], last ? z1[23] : z1[27], last ? z1[27] : z1[31]};
  }
  lds_wait(L, F_PAIR + (me ^ 1), round ? 7 : 1);
  if (pw) conv_send<0>(Lc, z0, lane);
  else conv_send<1>(Lc, z0, lane);
  lds_post(L, F_PAIR + me, round ? 15 : 3);
  lds_wait(L, F_PAIR + (me ^ 1), round ? 15 : 3);
  if (pw) conv_keep<1>(Lc, z0, lane);
  else conv_keep<0>(Lc, z0, lane);
#pragma unroll
  for (int n = 0; n < 2; ++n)
    st128<SC1>(*(LAS v4u*)(Lc + pw * 4096 + n * 1024 + lane * 16), mk(p.ws, WS_END), lane * 16 + n * 1024 + WS_YT + ch * 4096 + pw * 2048);
  ST(12);
}

__device__ __forceinline__ LAS char* s_buf(LAS char* L, int a) { return L + L_CONV + (a >> 1) * CONV_BUF + (a & 1) * 6144; }
__device__ __forceinline__ int xs_at(int a, int lane) { return (lane & 15) * 128 + (((2 * a + (lane >> 5)) ^ (lane & 7)) << 4) + ((lane >> 4) & 1) * 8; }
__device__ __forceinline__ void make_s(const Params& p, LAS char* L, const v2u (&xv)[NT], int t0, int G2, int a, u64 tag, int lane) {
  const int ch0 = 64 * G2 + 16 * a, i16 = lane & 15, g = lane >> 4;
  v2u xn[NT];
#pragma unroll
  for (int u = 0; u < NT; ++u) {
    const f32x2 st = *(LAS f32x2*)(L + L_STAT2 + (16 * u + i16) * 8);
    const float m = st.x, rd = st.y;
    const v2u x = xv[u];
    const unsigned d0 = pks(blo(x.x) - m, bhi(x.x) - m), d1 = pks(blo(x.y) - m, bhi(x.y) - m);
    xn[u] = v2u{pks(blo(d0) * rd, bhi(d0) * rd), pks(blo(d1) * rd, bhi(d1) * rd)};
    asm volatile("" : "+v"(xn[u]));
  }
  int yo[3];
#pragma unroll
  for (int n = 0; n < 3; ++n) {
    const int c = n * 64 + lane, row = c / 12;
    yo[n] = row * 4096 + (c - row * 12) * 16;
    asm volatile("" : "+v"(yo[n]));
  }
  SB();
  ST(13);
  wait_flag<6>(p, flag_at(FY + 2 * (ch0 + i16) + ((g & 1 ? t0 + NT - 1 : t0) >> 6)), tag);
  ST(11);
  LAS char* ys = s_buf(L, a);
#pragma unroll
  for (int n = 0; n < 3; ++n) dma16c(mkd(p.ws, WS_END), ys + n * 1024, yo[n], WS_YT + ch0 * 4096 + 32 * t0);
  WAIT_V(0);
  v2u y[NT];
#pragma unroll
  for (int u = 0; u < NT; ++u) y[u] = trread(ys + (4 * g + ((lane >> 2) & 3)) * 192 + u * 32 + (lane & 3) * 8);
  SB();
  WAIT_L0();
#pragma unroll
  for (int u = 0; u < NT; ++u)
    *(LAS v4u*)(ys + u * 1024 + lane * 16) = v4u{xv[u].x, xv[u].y, pks(blo(xn[u].x) * blo(y[u].x), bhi(xn[u].x) * bhi(y[u].x)),
                                                 pks(blo(xn[u].y) * blo(y[u].y), bhi(xn[u].y) * bhi(y[u].y))};
}

#define ASM_TS(n)
#define ASM_TC(n)
#define HDEL "2"
#define V10(a) "v" #a "0", "v" #a "1", "v" #a "2", "v" #a "3", "v" #a "4", "v" #a "5", "v" #a "6", "v" #a "7", "v" #a "8", "v" #a "9"
__device__ __forceinline__ void mma_waves(const Params& p, LAS char* L, int i, int j, bool job, int t0, int G2, int rot2, int w, int lane) {
  const int fh = w & 1, th = w >> 1, i16 = lane & 15, g = lane >> 4, q = (lane >> 2) & 3, Lb = (int)(size_t)L;
  const int fb = 128 * j + 64 * fh;
  v2u b1v[4];
#pragma unroll
  for (int m = 0; m < 4; ++m) b1v[m] = __builtin_amdgcn_raw_buffer_load_b64(mk(p.b1, F * 2), (fb + 16 * m + 4 * g) * 2, 0, 0);
  const v2u b2v = __builtin_amdgcn_raw_buffer_load_b64(mk(p.b2, D * 2), (64 * G2 + 16 * w + 4 * g) * 2, 0, 0);
  u64 ep = ask_epoch(p);
  SB();
  const int sb = 27 * th + 13 * fh, ss = 26 + 27 * th;
  i32x16 va, vb;
#pragma unroll
  for (int m = 0; m < 4; ++m) va[m] = Lb + L_W1 + (8 * g + q) * 256 + (((4 * fh + m) ^ (q | ((g & 1) << 2))) << 5) + 8 * (lane & 3);
#pragma unroll
  for (int ks = 0; ks < 2; ++ks) va[4 + ks] = Lb + (64 * th + i16) * 128 + (((4 * ks + g) ^ (i16 >> 1)) << 4);
  va[6] = Lb + L_STAT + (64 * th + i16) * 8;
  va[7] = Lb + L_CS + (64 * fh + 4 * g) * 4;
  va[8] = Lb + L_FLAG + w * 4;
  va[9] = Lb + L_FLAG + (lane & 3) * 4;
  va[10] = lane * 16 + WS_HT + (4 * j + 2 * fh) * HS + (8 * i + 4 * th) * 1024;
  va[11] = va[10] + HS;
  {
    int so[4];
    w2_src(so, lane);
#pragma unroll
    for (int h = 0; h < 4; ++h) va[12 + h] = so[h];
    const int rr = 4 * g + q;
#pragma unroll
    for (int dd = 0; dd < 4; ++dd) {
      vb[dd] = Lb + w * 4096 + rr * 128 + (((dd ^ w) ^ ((rr >> 1) & 3)) << 5) + 8 * (lane & 3);
      vb[4 + dd] = vb[dd] + 65536;
    }
    vb[8] = lane * 16 + fh * 3072;
    vb[9] = lane * 16 + 3072 - fh * 3072;
    vb[10] = lane == 0 ? flag_at(FH + (2 * i + th) * 32 + 2 * j + fh) : OOB;
    const int tl = min(t0 + 4 * (lane >> 3), t0 + NT - 1), c = lane & 7;
    vb[11] = flag_at(FH + (tl >> 2) * 32 + (c == 7 ? ss >> 1 : (sb >> 1) + c));
  }
  f32x32 acc[3];
  asm volatile(
      ".set n, 0\n"
      ".rept 32\n"
      " v_mov_b64 v[n:n+1], 0\n"
      " .set n, n+2\n"
      ".endr\n"
      : "={v[0:31]}"(acc[0]), "={v[32:63]}"(acc[1]), "+{v[128:143]}"(va));
  const int hb = WS_HT + t0 * 1024, wb = G2 * 128;
  int sh = hb + (sb + rot2) * HS, h0 = hb + sb * HS, h1 = h0 + 13 * HS, hs = hb + ss * HS;
  int sw = wb + g2_step(w, rot2, 8) * 40960, w0 = wb + sb * 40960, w1 = w0 + 13 * 40960, ws = wb + ss * 40960, w7 = wb + g2_step(w, rot2, 7) * 40960;
  int role = __builtin_amdgcn_readfirstlane((fb < F ? 1 : 0) + (job ? 2 : 0));
  asm volatile("" : "+s"(sh), "+s"(h0), "+s"(h1), "+s"(hs), "+s"(sw), "+s"(w0), "+s"(w1), "+s"(ws), "+s"(w7), "+s"(role));
  KvPre kp;
  kv_pre(kp, L + L_KV, L + L_KT, w, lane);
  BAR();
  asm volatile("" : "+s"(ep));
  kv_make(kp, 0);
  SB();
  f32x16 bb;
#pragma unroll
  for (int m = 0; m < 4; ++m) {
    bb[4 * m] = blo(b1v[m].x);
    bb[4 * m + 1] = bhi(b1v[m].x);
    bb[4 * m + 2] = blo(b1v[m].y);
    bb[4 * m + 3] = bhi(b1v[m].y);
  }
  vb[12] = b2v.x << 16;
  vb[13] = b2v.x & 0xffff0000u;
  vb[14] = b2v.y << 16;
  vb[15] = b2v.y & 0xffff0000u;
  {
    int cnt;
    asm volatile(
        ".macro G1RD n, s, sl, ks\n"
        " .if (\\n %% 3) == 2\n"
        "  ds_read_b128 v[\\s+16+4*(\\n/3):\\s+19+4*(\\n/3)], v[132+\\ks] offset:\\sl*16384+(\\n/3)*2048\n"
        " .else\n"
        "  ds_read_b64_tr_b16 v[\\s+4*(\\n/3)+2*(\\n%%3):\\s+4*(\\n/3)+2*(\\n%%3)+1], v[128+\\n/3] offset:\\sl*16384+\\ks*8192+(\\n%%3)*1024\n"
        " .endif\n"
        ".endm\n"
        ".macro STRD n\n"
        " .if \\n == 0\n"
        "  ds_read_b32 v71, v137 offset:%c21*4\n"
        " .elseif \\n < 5\n"
        "  ds_read_b64 v[70+2*\\n:71+2*\\n], v134 offset:(\\n-1)*128\n"
        " .else\n"
        "  ds_read_b128 v[60+4*\\n:63+4*\\n], v135 offset:(\\n-5)*64\n"
        " .endif\n"
        ".endm\n"
        ".macro G1H c, s, sl, ks\n"
        " .set n, 0\n"
        " .rept 16\n"
        "  v_mfma_f32_16x16x32_bf16 v[4*n:4*n+3], v[\\c+4*(n&3):\\c+4*(n&3)+3], v[\\c+16+4*(n/4):\\c+19+4*(n/4)], v[4*n:4*n+3]\n"
        "  .if (n < 12) && (\\sl >= 0)\n"
        "   G1RD n, \\s, \\sl, \\ks\n"
        "  .endif\n"
        "  .if (n >= 4) && (n < 13) && (\\sl < 0)\n"
        "   .set m, n-4\n"
        "   STRD m\n"
        "  .endif\n"
        "  .set n, n+1\n"
        " .endr\n"
        ".endm\n"
        ".macro LWAIT f\n"
        " s_mov_b32 %8, 0x400000\n"
        " 8:\n"
        " ds_read_b32 v70, v137 offset:\\f*4\n"
        " s_sub_u32 %8, %8, 1\n"
        " s_cbranch_scc1 9f\n"
        " s_waitcnt lgkmcnt(0)\n"
        " v_cmp_eq_u32 vcc, 0, v70\n"
        " s_cbranch_vccnz 8b\n"
        " 9:\n"
        ".endm\n"
        ".macro PK4 op, d, a, b, c, da, db, dc, mod:vararg\n"
        " .set e, 0\n"
        " .rept 4\n"
        "  .if \\c >= 0\n"
        "   \\op v[\\d+e:\\d+e+1], v[\\a+\\da*e:\\a+\\da*e+1], v[\\b+\\db*e:\\b+\\db*e+1], v[\\c+\\dc*e:\\c+\\dc*e+1] \\mod\n"
        "  .else\n"
        "   \\op v[\\d+e:\\d+e+1], v[\\a+\\da*e:\\a+\\da*e+1], v[\\b+\\db*e:\\b+\\db*e+1] \\mod\n"
        "  .endif\n"
        "  .set e, e+2\n"
        " .endr\n"
        ".endm\n"
        ".macro TR8 op\n"
        " .set e, 0\n"
        " .rept 8\n"
        "  \\op v[64+e], v[64+e]\n"
        "  .set e, e+1\n"
        " .endr\n"
        ".endm\n"
        ".macro HST y\n"
        " buffer_store_dwordx4 v[16*(\\y&3)+8*(\\y/4):16*(\\y&3)+8*(\\y/4)+3], v[138+\\y/4], %9, 0 offen offset:(\\y&3)*1024 sc1\n"
        ".endm\n"
        ".macro PUBLISH\n"
        " s_waitcnt vmcnt(0)\n"
        " buffer_store_dwordx2 v[68:69], v250, %9, 0 offen sc1\n"
        ".endm\n"
        ".macro G2RD k, n\n"
        " .set b, (0x53024176 >> (4*(\\k&7))) & 7\n"
        " ds_read_b64_tr_b16 v[144+16*(\\k%%6)+2*\\n:145+16*(\\k%%6)+2*\\n], v[240+4*(b/4)+\\n/2] offset:(b&3)*16384+(\\n&1)*2048\n"
        ".endm\n"
        ".macro W2LD k, so\n"
        " s_add_u32 m0, %19, ((0x53024176 >> (4*(\\k&7))) & 7)*16384\n"
        " s_nop 0\n"
        " buffer_load_dwordx4 v140, %10, \\so offen lds\n"
        " buffer_load_dwordx4 v141, %10, \\so offen offset:1024 lds\n"
        " buffer_load_dwordx4 v142, %10, \\so offen offset:2048 lds\n"
        " buffer_load_dwordx4 v143, %10, \\so offen offset:3072 lds\n"
        ".endm\n"
        ".macro G2LD q\n"
        " .set lu, \\q %% 6\n"
        " .if \\q >= 78\n"
        "  .set lu, \\q - 75\n"
        " .endif\n"
        " buffer_load_dwordx4 v[96+4*(\\q%%12):99+4*(\\q%%12)], v[248+lu/3], %9, %6 offen offset:(lu%%3)*1024 sc1\n"
        ".endm\n"
        ".macro G2MF d\n"
        " .if k > 0\n"
        "  v_mfma_f32_16x16x32_bf16 v[16*u+4*\\d:16*u+4*\\d+3], v[144+16*(k%%6)+4*\\d:147+16*(k%%6)+4*\\d], v[96+4*(q%%12):99+4*(q%%12)], "
        "v[16*u+4*\\d:16*u+4*\\d+3]\n"
        " .elseif \\d > 0\n"
        "  v_mfma_f32_16x16x32_bf16 v[16*u+4*\\d:16*u+4*\\d+3], v[144+4*\\d:147+4*\\d], v[96+4*q:99+4*q], 0\n"
        " .else\n"
        "  v_mfma_f32_16x16x32_bf16 v[16*u:16*u+3], v[144:147], v[96+4*q:99+4*q], v[252:255]\n"
        " .endif\n"
        ".endm\n"
        "v_mov_b32 v160, 0xbdd2d3e8\n"
        "v_mov_b32 v161, 0xc0135761\n"
        "v_mov_b32 v162, 1.0\n"
        "v_mov_b32 v163, 1\n"
        "s_mov_b32 %8, 3\n"
        "s_waitcnt lgkmcnt(0)\n"
        "s_barrier\n" ASM_TS(0) ASM_TC(8)
        ".set n, 0\n"
        ".rept 12\n"
        " G1RD n, 64, 0, 0\n"
        " .set n, n+1\n"
        ".endr\n"
        "1:\n"
        "s_waitcnt lgkmcnt(0)\n"
        "G1H 64, 96, 0, 1\n"
        "s_cmp_eq_u32 %8, 0\n"
        "s_waitcnt lgkmcnt(0)\n"
        "s_cbranch_scc1 2f\n"
        "s_barrier\n"
        "G1H 96, 64, 1, 0\n"
        "s_waitcnt lgkmcnt(0)\n"
        "G1H 64, 96, 1, 1\n"
        "s_waitcnt lgkmcnt(0)\n"
        "s_barrier\n"
        "G1H 96, 64, 2, 0\n"
        "s_waitcnt lgkmcnt(0)\n"
        "G1H 64, 96, 2, 1\n"
        "s_waitcnt lgkmcnt(0)\n"
        "s_barrier\n"
        "s_sub_u32 %8, %8, 1\n"
        "G1H 96, 64, 0, 0\n"
        "s_branch 1b\n"
        "3:\n"
        "LWAIT %c21\n"
        ".set m, 1\n"
        ".rept 8\n"
        " STRD m\n"
        " .set m, m+1\n"
        ".endr\n"
        "s_waitcnt lgkmcnt(0)\n"
        "s_branch 4f\n"
        "2:\n"
        "ds_write_b32 v136, v163 offset:%c23*4\n"
        "G1H 96, 64, -1, 0\n"
        "s_setprio 2\n" ASM_TS(1) ASM_TC(9)
        "s_bitcmp1_b32 %20, 0\n"
        "s_cbranch_scc0 5f\n"
        "s_waitcnt lgkmcnt(0)\n"
        "v_cmp_eq_u32 vcc, 0, v71\n"
        "s_cbranch_vccnz 3b\n"
        "4:\n" ASM_TS(2)
        ".set y, 0\n"
        ".rept 8\n"
        " .set a, 16*(y&3)+8*(y/4)\n"
        " .set t, 72+2*(y&3)\n"
        " PK4 v_pk_fma_f32, a, t, 80+8*(y/4), a, 0, 1, 1, op_sel_hi:[0,1,1] neg_lo:[1,0,0] neg_hi:[1,0,0]\n"
        " .if y >= " HDEL "\n"
        "  .set yp, y-" HDEL "\n"
        "  HST yp\n"
        " .endif\n"
        " PK4 v_pk_fma_f32, a, a, t, 144+8*(y/4), 1, 0, 1, op_sel:[0,1,0]\n"
        " PK4 v_pk_mul_f32, 64, a, a, -1, 1, 1, 0\n"
        " PK4 v_pk_fma_f32, 64, 64, 160, 160, 1, 0, 0, op_sel:[0,0,1] op_sel_hi:[1,0,1]\n"
        " PK4 v_pk_mul_f32, 64, a, 64, -1, 1, 1, 0\n"
        " TR8 v_exp_f32\n"
        " PK4 v_pk_add_f32, 64, 64, 162, -1, 1, 0, 0, op_sel_hi:[1,0]\n"
        " TR8 v_rcp_f32\n"
        " PK4 v_pk_mul_f32, a, a, 64, -1, 1, 1, 0\n"
        " v_cvt_pk_bf16_f32 v[a], v[a], v[a+1]\n"
        " v_cvt_pk_bf16_f32 v[a+1], v[a+2], v[a+3]\n"
        " v_cvt_pk_bf16_f32 v[a+2], v[a+4], v[a+5]\n"
        " v_cvt_pk_bf16_f32 v[a+3], v[a+6], v[a+7]\n"
        " .set y, y+1\n"
        ".endr\n"
        ".set yp, 8-" HDEL "\n"
        ".rept " HDEL "\n"
        " HST yp\n"
        " .set yp, yp+1\n"
        ".endr\n"
        "5:\n"
        "ds_write_b32 v136, v163 offset:%c22*4\n"
        "ds_read_b32 v70, v137 offset:%c24*4\n" ASM_TS(3)
        "v_mov_b64 v[68:69], %18\n"
        "s_bitcmp1_b32 %20, 1\n"
        "s_waitcnt lgkmcnt(0)\n"
        "v_cmp_eq_u32 vcc, 0, v70\n"
        "s_cselect_b64 vcc, vcc, -1\n"
        "s_cbranch_vccz 6f\n"
        "PUBLISH\n"
        "s_cbranch_scc0 7f\n"
        "v_mov_b32 v250, 2.0\n"
        "LWAIT %c24\n"
        "6:\n"
        ".set q, 0\n"
        ".rept 48\n"
        " .set k, q/8\n"
        " .set n, q&7\n"
        " G2RD k, n\n"
        " .set q, q+1\n"
        ".endr\n"
        "PUBLISH\n" ASM_TS(4)
        "s_waitcnt lgkmcnt(0)\n"
        ".set k, 8\n"
        ".rept 5\n"
        " W2LD k, %7\n"
        " s_add_u32 %7, %7, 40960\n"
        " s_cmp_eq_u32 %7, %15\n"
        " s_cselect_b32 %7, %14, %7\n"
        " .set k, k+1\n"
        ".endr\n"
        "W2LD 13, %16\n"
        "LWAIT 4\n"
        "W2LD 7, %17\n" ASM_TS(5)
        "s_mov_b32 %8, 0x8000\n"
        "8:\n"
        "buffer_load_dwordx2 v[70:71], v251, %9, 0 offen sc1\n"
        "s_sub_u32 %8, %8, 1\n"
        "s_cbranch_scc1 9f\n"
        "s_waitcnt vmcnt(0)\n"
        "v_cmp_ne_u64 vcc, %18, v[70:71]\n"
        "s_cbranch_vccnz 8b\n"
        "9:\n" ASM_TS(6)
        ".set q, 0\n"
        ".rept 11\n"
        " G2LD q\n"
        " .if q == 5\n"
        "  s_add_u32 %6, %6, %c25\n"
        "  s_cmp_eq_u32 %6, %12\n"
        "  s_cselect_b32 %6, %11, %6\n"
        " .endif\n"
        " .set q, q+1\n"
        ".endr\n"
        "s_setprio 0\n"
        ".set q, 0\n"
        ".rept 81\n"
        " .set k, q/6\n"
        " .set u, q%%6\n"
        " .if q >= 78\n"
        "  .set u, q-75\n"
        " .endif\n"
        " .set o, 10\n"
        " .if q > 70\n"
        "  .set o, 80-q\n"
        " .endif\n"
        " .if (q%%6) == 0\n"
        "  s_waitcnt vmcnt(o) lgkmcnt(0)\n"
        " .else\n"
        "  s_waitcnt vmcnt(o)\n"
        " .endif\n"
        " G2MF 0\n"
        " .set ql, q+11\n"
        " .if ql < 81\n"
        "  G2LD ql\n"
        " .endif\n"
        " G2MF 1\n"
        " .if (u == 0) && (ql < 77)\n"
        "  s_add_u32 %6, %6, %c25\n"
        "  s_cmp_eq_u32 %6, %12\n"
        " .endif\n"
        " .if ql == 77\n"
        "  s_mov_b32 %6, %13\n"
        " .endif\n"
        " G2MF 2\n"
        " .if (u == 0) && (ql < 77)\n"
        "  s_cselect_b32 %6, %11, %6\n"
        " .endif\n"
        " .set kr, k+5\n"
        " .if (u >= 1) && (u <= 4) && (k >= 1) && (k <= 8)\n"
        "  .set n, 2*u-2\n"
        "  G2RD kr, n\n"
        " .endif\n"
        " G2MF 3\n"
        " .if (u >= 1) && (u <= 4) && (k >= 1) && (k <= 8)\n"
        "  .set n, 2*u-1\n"
        "  G2RD kr, n\n"
        " .endif\n"
        " .set q, q+1\n"
        ".endr\n"
        "s_nop 7\n"
        "7:\n"
        ".purgem G1RD\n .purgem STRD\n .purgem G1H\n .purgem LWAIT\n .purgem PK4\n .purgem TR8\n .purgem HST\n .purgem PUBLISH\n"
        ".purgem G2RD\n .purgem W2LD\n .purgem G2LD\n .purgem G2MF\n"
        : "+{v[0:31]}"(acc[0]), "+{v[32:63]}"(acc[1]), "=&{v[64:95]}"(acc[2]), "+{v[128:143]}"(va), "+{v[144:159]}"(bb), "+{v[240:255]}"(vb),
          "+&s"(sh), "+&s"(sw), "=&s"(cnt)
        : "s"(mkd(p.ws, WS_END)), "s"(mkd(p.w2, F * D * 2)), "s"(h0), "s"(h1), "s"(hs), "s"(w0), "s"(w1), "s"(ws), "s"(w7), "s"(tag_of(ep)),
          "s"(Lb + w * 4096), "s"(role), "n"(F_STAT), "n"(F_GELU), "n"(F_RING), "n"(F_W2), "n"(HS)
        : "memory", "scc", "vcc", "v96", "v97", "v98", "v99", V10(10), V10(11), "v120", "v121", "v122", "v123", "v124", "v125", "v126", "v127",
          V10(16), V10(17), V10(18), V10(19), V10(20), V10(21), V10(22), V10(23)
    );
  }
  if (!job) return;
  ST(5);
  const int ln = lane_id();
  auto tile = [&](int u, int dd) {
    const int b = (16 * u + 4 * dd) & 31;
    return f32x4{acc[u >> 1][b], acc[u >> 1][b + 1], acc[u >> 1][b + 2], acc[u >> 1][b + 3]};
  };
#pragma unroll
  for (int u = 0; u < NT; ++u)
#pragma unroll
    for (int dd = 1; dd < 4; ++dd) *(LAS f32x4*)(L + (2 * dd - 2 + u / 3) * 16384 + w * 4096 + (u % 3) * 1024 + ln * 16) = tile(u, dd);
  BAR();
  ST(6);
#pragma unroll
  for (int u = 0; u < NT; ++u) {
    f32x4 z = tile(u, 0);
#pragma unroll
    for (int dd = 1; dd < 4; ++dd) z += *(LAS f32x4*)(L + (2 * dd - 2 + ((u / 3) ^ (dd & 1))) * 16384 + (w ^ dd) * 4096 + (u % 3) * 1024 + ln * 16);
    const int T = u < 3 ? u + 3 * (w & 1) : u - 3 * (w & 1);
    const v4u xs = *(LAS v4u*)(s_buf(L, w) + T * 1024 + ln * 16);
    const float xx[4] = {blo(xs.x), bhi(xs.x), blo(xs.y), bhi(xs.y)}, ss2[4] = {blo(xs.z), bhi(xs.z), blo(xs.w), bhi(xs.w)};
    float o[4];
#pragma unroll
    for (int r = 0; r < 4; ++r) {
      const float gt = z[r] * __builtin_amdgcn_rcpf(1.f + __builtin_amdgcn_exp2f(z[r] * -1.442695041f));
      o[r] = fmaf(ss2[r], gt, xx[r]);
    }
    *(LAS v2u*)(L + L_OUT + xs_at(w, ln) + T * 2048) = v2u{pk(o[0], o[1]), pk(o[2], o[3])};
  }
}

__device__ __forceinline__ void store_out(const Params& p, LAS char* L, int t0, int rng, int G2, int w) {
  BAR();
  const int ln = lane_id();
#pragma unroll
  for (int h = 0; h < 2; ++h) {
    const int pc = w + 8 * h;
    if (pc < 12 && 2 * t0 + pc >= 12 * rng)
      st128<0>(*(LAS v4u*)(L + L_OUT + pc * 1024 + ln * 16), mk(p.out, S * D * 2),
               (16 * t0 + 8 * pc + (ln >> 3)) * 1280 + G2 * 128 + (((ln & 7) ^ (ln >> 3)) << 4));
  }
  ST(7);
}

constexpr int P_KT = 77312;
__device__ __forceinline__ float dot2(unsigned a, unsigned b, float c) {
  return __builtin_amdgcn_fdot2_f32_bf16(__builtin_bit_cast(bf2_t, a), __builtin_bit_cast(bf2_t, b), c, false);
}
__device__ __forceinline__ float sum8(float v) {
  v += __int_as_float(__builtin_amdgcn_mov_dpp(__float_as_int(v), 0xb1, 0xf, 0xf, true));
  v += __int_as_float(__builtin_amdgcn_mov_dpp(__float_as_int(v), 0x4e, 0xf, 0xf, true));
  return v + __int_as_float(__builtin_amdgcn_mov_dpp(__float_as_int(v), 0x141, 0xf, 0xf, true));
}
__device__ __forceinline__ u64 prep_kv(const Params& p, LAS char* L, int pp, int w, int lane) {
  {
    const int row = lane >> 3, o = (64 * pp + 32 * (w & 1) + row) * 1280 + 7 * 128 + (((lane & 7) ^ row) << 4);
    const int vo[4] = {o, o + 9216, o + 2 * 9216, o + 3 * 9216};
    const v4u rk = mkd(w >= 2 ? p.v : p.k, S * D * 2);
#pragma unroll
    for (int n = 0; n < 3; ++n) {
      dma4(rk, L + n * KV_IN + 1024 + w * 4096, vo, n * 128);
      if (w == 0) dma16(rk, L + n * KV_IN, row >= 5 ? o - 8 * 1280 : OOB, n * 128);
      if (w == 3) dma16(mkd(p.sk, D * 8), L + n * KV_IN + 17408, lane < 32 ? lane * 16 : OOB, (7 + n) * 512);
    }
  }
  TS(0);
  __builtin_amdgcn_s_setprio(3);
  const int c0 = 448 + 6 * pp;
  u64 ep;
  unsigned d0, d1;
  asm volatile("s_load_dwordx2 %0, %3, 0x0\n\ts_load_dword %1, %4, 0x0\n\ts_load_dword %2, %5, 0x8\n\ts_waitcnt lgkmcnt(0)"
               : "=&s"(ep), "=&s"(d0), "=&s"(d1)
               : "s"(p.ws + WS_EPOCH), "s"(p.dv + (c0 + (w & 2)) * 2), "s"(p.dv + c0 * 2)
               : "memory");
  const u64 tag = tag_of(ep);
  d0 = w & 1 ? d0 & 0xffff0000u : d0 << 16;
  d1 = w & 1 ? d1 & 0xffff0000u : d1 << 16;
  KvPre kp;
  kv_pre(kp, L, L + P_KT, w, lane);
  WAIT_V(0);
  lds_post(L, F_GELU + w, 1);
  lds_wait4(L, F_GELU, lane);
#pragma unroll
  for (int n = 0; n < 3; ++n) kv_make(kp, n);
  TS(1);
  lds_post(L, F_RING + w, 1);
  lds_wait4(L, F_RING, lane);
  kv_store<4>(p, L + P_KT, 4 * w, pp, 7, lane);
  if (w < 2) kv_store<4>(p, L + P_KT, 16 + 4 * w, pp, 7, lane);
  TS(2);
  WAIT_V(0);
  set_flag(p, lane < (w < 2 ? 2 : 1) ? flag_at(FK + (14 + w + 4 * lane) * 32 + pp) : OOB, tag);
  TS(3);
  LAS char* L0 = L + w * CONV_BUF;
  LAS char* L1 = L0 + 4 * CONV_BUF;
  stage_h<0>(p, L0, (c0 + w) * 4096 + lane * 16);
  stage_h<1>(p, L0, (c0 + w) * 4096 + lane * 16);
  stage_pads(L0, lane);
  if (w < 2) {
    stage_h<0>(p, L1, (c0 + 4 + w) * 4096 + lane * 16);
    stage_h<1>(p, L1, (c0 + 4 + w) * 4096 + lane * 16);
    stage_pads(L1, lane);
  }
  wait_flag<2>(p, flag_at(FK + ((c0 + (lane < 32 ? 0 : 5)) >> 5) * 32 + (lane & 31)), tag);
  TS(4);
  stage_u(p, L0, c0 + w, lane);
  stage_patch(L0, d0);
  if (w < 2) {
    stage_u(p, L1, c0 + 4 + w, lane);
    stage_patch(L1, d1);
  }
  WAIT_V(0);
  lds_post(L, F_STAGED + w, 1);
  TS(5);
  return tag;
}
__device__ __forceinline__ u64 prep_stats(const Params& p, LAS char* L, int pp, int a, int lane) {
  const rsrc_t rx = mk(p.x, S * D * 2), rws = mk(p.ws, WS_END);
  const int row = lane >> 3, c = lane & 7;
  v4u xr[20];
#pragma unroll
  for (int n = 0; n < 20; ++n) xr[n] = __builtin_amdgcn_raw_buffer_load_b128(rx, (64 * pp + 16 * a + row) * 1280 + c * 16 + (n % 10) * 128, (n / 10) * 8 * 1280, 0);
  SB();
  TS(0);
  const u64 tag = get_tag(p);
  const unsigned t32 = (unsigned)(tag >> 32);
  {
    float sx[2], sq[2];
#pragma unroll
    for (int h = 0; h < 2; ++h) {
      sx[h] = sq[h] = 0.f;
#pragma unroll
      for (int n = 0; n < 40; ++n) {
        const unsigned v = xr[10 * h + (n >> 2)][n & 3];
        sx[h] = dot2(v, 0x3f803f80u, sx[h]);
        sq[h] = dot2(v, v, sq[h]);
      }
      sx[h] = sum8(sx[h]);
      sq[h] = sum8(sq[h]);
    }
    const float mean = (c ? sx[1] : sx[0]) * (1.f / D);
    const float var = fmaxf((c ? sq[1] : sq[0]) * (1.f / D) - mean * mean, 0.f);
    const unsigned md = pk(mean, __builtin_amdgcn_sqrtf(rb(rb(var) + 1e-5f)));
    __builtin_amdgcn_raw_buffer_store_b64(v2u{md, t32}, rws, c < 2 ? WS_STAT + (64 * pp + 16 * a + 8 * c + row) * 8 : OOB, 0, SC1);
  }
  SB();
  TS(1);
  return tag;
}

__global__ __launch_bounds__(512, 2) void h3_kernel(Params p) {
  extern __shared__ __attribute__((aligned(16))) char smem[];
  LAS char* L = (LAS char*)smem;
  const int lane = threadIdx.x & 63, w = __builtin_amdgcn_readfirstlane(threadIdx.x >> 6);
  const unsigned bid = blockIdx.x, xcd = bid & 7, r = bid >> 3;
  ST(0);
  if (w >= 6) code_prefetch(__builtin_amdgcn_s_getpc() & ~255ull, L + L_DUMP, r + 32 * (w - 6), lane);
  SB();
  const bool g1 = r < 28;
  const int jb = 28 * xcd + r, jx = 28 * (xcd >> 1) + r, rng = (jx * 205) >> 10, g5 = jx - 5 * rng, G2 = 5 * (xcd & 1) + g5, t0 = min(6 * rng, 122);
  const bool job = jx < 110;
  const int rot2 = (((g5 + rng) % 5) * 21) >> 3;
  const int rr = (r * 37) >> 8, i = 4 * (xcd >> 1) + rr, j = 7 * (xcd & 1) + (r - 7 * rr);
  u64 tag;
  int ch, nround = 1, gate = -1;
  LAS char* Lc;
  v2u xv[NT];
  if (__builtin_expect(g1, 1)) {
    if (__builtin_expect(w < 4, 0)) {
      mma_waves(p, L, i, j, job, t0, G2, rot2, w, lane);
      if (job) store_out(p, L, t0, rng, G2, w);
      DUMP();
      return;
    }
    __builtin_amdgcn_s_setprio(3);
    if (__builtin_expect(w < 6, 1)) tag = gemm1_load(p, L, i, j, job, G2, rot2, jb, w - 4, lane);
    else tag = gemm1_side(p, L, i, j, t0, G2, jb, w - 6, lane);
    lds_wait4(L, F_STAGED, lane);
#pragma unroll
    for (int u = 0; u < NT; ++u) xv[u] = *(LAS v2u*)(L + L_XS + xs_at(w - 4, lane) + u * 2048);
    ch = 2 * jb + ((w >> 1) & 1);
    gate = F_GELU + (w & 3);
    Lc = L + L_CONV + ((w >> 1) & 1) * CONV_BUF;
  } else {
    const int pp = 4 * xcd + (r - 28);
    if (w == 7) *(LAS int*)(L + L_FLAG + lane * 4) = 0;
    BAR();
    if (w >= 4) __builtin_amdgcn_s_setprio(3);
    if (w < 4) tag = prep_kv(p, L, pp, w, lane);
    else tag = prep_stats(p, L, pp, w - 4, lane);
    ST(3);
    lds_wait4<2>(L, F_STAGED, lane);
    ch = 448 + 6 * pp + (w >> 1);
    nround = w < 4 ? 2 : 1;
    Lc = L + (w >> 1) * CONV_BUF;
  }
#pragma unroll 1
  for (int round = 0; round < nround; ++round) {
    conv(p, L, Lc, ch, w, round, gate, tag, lane);
    Lc += 4 * CONV_BUF;
    ch += 4;
  }
  publish_y(p, ch - 4, w & 1, tag, lane);
  ST(4);
  if (g1 && job) {
    lds_post(L, F_PAIR + w, 7);
    lds_wait(L, F_PAIR + (w ^ 1), 7);
    make_s(p, L, xv, t0, G2, w - 4, tag, lane);
    ST(5);
    BAR();
    store_out(p, L, t0, rng, G2, w);
  } else if (bid == 255 && w == 7) {
#pragma unroll 1
    for (int spin = 0; spin < SPIN_CAP; ++spin) {
      bool ok = true;
#pragma unroll
      for (int n = 0; n < 12; ++n) {
        const v2u f = __builtin_amdgcn_raw_buffer_load_b64(mk(p.ws, WS_END), lane * 128, n < 10 ? flag_at(FK + n * 64) : WS_STAT + (n - 10) * 8192, SC1);
        ok &= f.y == (unsigned)(tag >> 32) && (n >= 10 || f.x == (unsigned)tag);
      }
      if (__all(ok)) break;
      asm volatile("s_sleep 8" ::: "memory");
    }
    set_flag(p, lane == 0 ? WS_EPOCH : OOB, tag);
  }
  DUMP();
}
}

extern "C" void kernel_launch(void* const* d_in, const int* in_sizes, int n_in, void* d_out, int out_size, void* d_ws, size_t ws_size,
                              hipStream_t stream) {
  using namespace h3;
  Params p = {};
  p.x = (const char*)d_in[0];
  p.k = (const char*)d_in[1];
  p.v = (const char*)d_in[2];
  p.sk = (const char*)d_in[3];
  p.hk = (const char*)d_in[4];
  p.dv = (const char*)d_in[5];
  p.w1 = (const char*)d_in[7];
  p.b1 = (const char*)d_in[8];
  p.w2 = (const char*)d_in[9];
  p.b2 = (const char*)d_in[10];
  p.out = (char*)d_out;
  p.ws = (char*)d_ws;
  (void)hipFuncSetAttribute((const void*)h3_kernel, hipFuncAttributeMaxDynamicSharedMemorySize, LDS_BYTES);
  h3_kernel<<<256, 512, LDS_BYTES, stream>>>(p);
}
```

```cpp
#include <hip/hip_runtime.h>
#include <stdint.h>

namespace h3 {
#define LAS __attribute__((address_space(3)))
#define GAS __attribute__((address_space(1)))
typedef unsigned v4u __attribute__((ext_vector_type(4)));
typedef unsigned v2u __attribute__((ext_vector_type(2)));
typedef float f32x4 __attribute__((ext_vector_type(4)));
typedef float f32x2 __attribute__((ext_vector_type(2)));
typedef float f32x16 __attribute__((ext_vector_type(16)));
typedef float f32x32 __attribute__((ext_vector_type(32)));
typedef unsigned u32x32 __attribute__((ext_vector_type(32)));
typedef int i32x16 __attribute__((ext_vector_type(16)));
typedef short s16x8 __attribute__((ext_vector_type(8)));
typedef short s16x4 __attribute__((ext_vector_type(4)));
typedef __bf16 bf2_t __attribute__((ext_vector_type(2)));
typedef unsigned long long u64;
typedef __amdgpu_buffer_rsrc_t rsrc_t;

constexpr int S = 2048, D = 640, F = 1728, KS2 = 54, NT = 6;
constexpr int SC1 = 16;
#define LAYOUT5
#define LAYOUT11
#define LAYOUT15
constexpr int WS_EPOCH = 0;
constexpr int WS_STAT = 4096;
constexpr int WS_CS = 20480;
constexpr int HS = 129 * 1024;
constexpr int WS_HT = 196608;
constexpr int WS_KVT = WS_HT + KS2 * HS;
constexpr int YS = 22 * 256;
constexpr int WS_YT = WS_KVT + D * S * 2;
constexpr int WS_FL = WS_YT + D * YS;
constexpr int FSTR = 128;
constexpr int FH = 0;
constexpr int FK = 1024;
constexpr int WS_END = WS_FL + 2304 * FSTR;
__device__ __forceinline__ int flag_at(int n) { return WS_FL + n * FSTR; }
constexpr int WS_STAMP = 32 << 20;
constexpr int OOB = 0x40000000;
constexpr int L_W1 = 49152;
constexpr int L_KV = 98304, L_KT = L_KV + 18432;
constexpr int L_XS = 98304;
constexpr int C_A2 = 4128, C_B = 8256, C_BS = 1184, CONV_BUF = C_B + 4 * C_BS;
constexpr int L_CONV = 131072;
constexpr int L_STAT = L_CONV + 2 * CONV_BUF, L_CS = L_STAT + 1024, L_STAT2 = L_CS + 512, L_FLAG = L_STAT2 + 768, L_DUMP = L_FLAG + 256, L_TAG = L_DUMP + 1024, L_B2 = L_TAG + 16;
constexpr int LDS_BYTES = 163840;
static_assert(L_DUMP + 2048 <= LDS_BYTES, "lds");
constexpr int F_PAIR = 0, F_STAT = 12, F_GELU = 16, F_RING = 20, F_STAGED = 24, F_DONE = 32;
#define CODE_BYTES 32768
constexpr int SPIN_CAP = 1 << 15;

struct Params {
  const char *x, *k, *v, *sk, *hk, *dv, *w1, *b1, *w2, *b2;
  char *out, *ws;
};

__device__ __forceinline__ rsrc_t mk(const void* p, int bytes) { return __builtin_amdgcn_make_buffer_rsrc((void*)p, 0, bytes, 0x00020000); }
__device__ __forceinline__ f32x4 mfma(v4u a, v4u b, f32x4 c) {
  return __builtin_amdgcn_mfma_f32_16x16x32_bf16(__builtin_bit_cast(s16x8, a), __builtin_bit_cast(s16x8, b), c, 0, 0, 0);
}
__device__ __forceinline__ v2u trread(LAS char* a) { return __builtin_bit_cast(v2u, __builtin_amdgcn_ds_read_tr16_b64_v4i16((LAS s16x4*)a)); }
__device__ __forceinline__ v4u cat(v2u a, v2u b) { return v4u{a.x, a.y, b.x, b.y}; }
__device__ __forceinline__ float blo(unsigned u) { return __uint_as_float(u << 16); }
__device__ __forceinline__ float bhi(unsigned u) { return __uint_as_float(u & 0xffff0000u); }
__device__ __forceinline__ unsigned pk(float a, float b) {
  bf2_t r;
  r[0] = (__bf16)a;
  r[1] = (__bf16)b;
  return __builtin_bit_cast(unsigned, r);
}
__device__ __forceinline__ float rb(float a) { return blo(pk(a, a)); }
__device__ __forceinline__ unsigned pks(float a, float b) {
  asm("" : "+v"(a));
  asm("" : "+v"(b));
  return pk(a, b);
}
__device__ __forceinline__ v4u mkd(const void* p, int bytes) {
  const u64 a = (u64)p;
  return v4u{(unsigned)a, (unsigned)(a >> 32) & 0xffffu, (unsigned)bytes, 0x00020000u};
}
__device__ __forceinline__ void dma16(v4u rs, LAS char* dst, int voff, int soff) {
  asm volatile("s_mov_b32 m0, %0\n\ts_nop 0\n\tbuffer_load_dwordx4 %1, %2, %3 offen lds" ::"s"((unsigned)(size_t)dst), "v"(voff), "s"(rs), "s"(soff)
               : "memory");
}
__device__ __forceinline__ void dma16c(v4u rs, LAS char* dst, int voff, int soff) {
  asm volatile("s_mov_b32 m0, %0\n\ts_nop 0\n\tbuffer_load_dwordx4 %1, %2, %3 offen sc1 lds" ::"s"((unsigned)(size_t)dst), "v"(voff), "s"(rs), "s"(soff)
               : "memory");
}
__device__ __forceinline__ void dma4(v4u rs, LAS char* dst, const int (&vo)[4], int soff) {
  asm volatile(
      "s_mov_b32 m0, %0\n\ts_nop 0\n\tbuffer_load_dwordx4 %1, %5, %6 offen lds\n\tbuffer_load_dwordx4 %2, %5, %6 offen offset:1024 lds\n\t"
      "buffer_load_dwordx4 %3, %5, %6 offen offset:2048 lds\n\tbuffer_load_dwordx4 %4, %5, %6 offen offset:3072 lds" ::"s"((unsigned)(size_t)dst),
      "v"(vo[0]), "v"(vo[1]), "v"(vo[2]), "v"(vo[3]), "s"(rs), "s"(soff)
      : "memory");
}
template <int AUX>
__device__ __forceinline__ void st128(v4u v, rsrc_t rs, int voff) { __builtin_amdgcn_raw_buffer_store_b128(v, rs, voff, 0, AUX); }
#define WAIT_V(n) asm volatile("s_waitcnt vmcnt(" #n ")" ::: "memory")
#define WAIT_L0() asm volatile("s_waitcnt lgkmcnt(0)" ::: "memory")
#define BAR()                          \
  do {                                 \
    WAIT_L0();                         \
    __builtin_amdgcn_s_barrier();      \
    asm volatile("" ::: "memory");     \
  } while (0)
#define SB() __builtin_amdgcn_sched_barrier(0)
#define RAW(idx, v)
#define ST(idx)
#define TS(k)
#define DUMP()

__device__ __forceinline__ int lane_id() {
  int l;
  asm volatile("v_mbcnt_lo_u32_b32 %0, -1, 0\n\tv_mbcnt_hi_u32_b32 %0, -1, %0" : "=v"(l));
  return l;
}
template <int NAP = 0>
__device__ __forceinline__ void wait_flag(const Params& p, int off, u64 tag) {
  int cnt = SPIN_CAP;
  u64 f;
  asm volatile(
      "1:\n"
      "buffer_load_dwordx2 %0, %2, %3, 0 offen sc1\n"
      "s_sub_u32 %1, %1, 1\n"
      "s_cbranch_scc1 2f\n"
      "s_sleep %5\n"
      "s_waitcnt vmcnt(0)\n"
      "v_cmp_ne_u64 vcc, %4, %0\n"
      "s_mov_b64 exec, vcc\n"
      "s_cbranch_vccnz 1b\n"
      "2:\n"
      "s_mov_b64 exec, -1\n"
      "s_waitcnt vmcnt(0)\n"
      : "=&v"(f), "+s"(cnt)
      : "v"(off), "s"(mkd(p.ws, WS_END)), "s"(tag), "n"(NAP)
      : "memory", "vcc", "scc");
}
__device__ __forceinline__ void set_flag(const Params& p, int off, u64 tag) {
  __builtin_amdgcn_raw_buffer_store_b64(v2u{(unsigned)tag, (unsigned)(tag >> 32)}, mk(p.ws, WS_END), off, 0, SC1);
}
__device__ __forceinline__ void lds_post(LAS char* L, int idx, int val) {
  asm volatile("" ::: "memory");
  *(volatile LAS int*)(L + L_FLAG + idx * 4) = val;
}
__device__ __forceinline__ void lds_post2(LAS char* L, int idx) {
  asm volatile("" ::: "memory");
  *(volatile LAS v2u*)(L + L_FLAG + idx * 4) = v2u{1u, 1u};
}
template <int NAP = 0, int OFF = 0>
__device__ __forceinline__ void lds_wait_at(int a, int val) {
  int cnt = 1 << 22, f;
  asm volatile(
      "1:\n"
      "ds_read_b32 %0, %2 offset:%5\n"
      "s_sub_u32 %1, %1, 1\n"
      "s_cbranch_scc1 2f\n"
      ".if %4\n s_sleep %4\n .endif\n"
      "s_waitcnt lgkmcnt(0)\n"
      "v_cmp_gt_i32 vcc, %3, %0\n"
      "s_cbranch_vccnz 1b\n"
      "2:\n"
      "s_waitcnt lgkmcnt(0)\n"
      : "=&v"(f), "+s"(cnt)
      : "v"(a), "s"(val), "n"(NAP), "n"(OFF)
      : "memory", "vcc", "scc");
}
template <int NAP = 0>
__device__ __forceinline__ void lds_wait(LAS char* L, int idx, int val) { lds_wait_at<NAP>((int)(size_t)(L + L_FLAG + idx * 4), val); }
template <int NAP = 0>
__device__ __forceinline__ void lds_wait4(LAS char* L, int idx, int lane) { lds_wait<NAP>(L, idx + (lane & 3), 1); }
__device__ __forceinline__ u64 tag_of(u64 e) { return e * 6364136223846793005ull + 1442695040888963407ull; }
__device__ __forceinline__ u64 ask_epoch(const Params& p) {
  u64 e;
  asm volatile("s_load_dwordx2 %0, %1, 0x0" : "=s"(e) : "s"(p.ws + WS_EPOCH) : "memory");
  return e;
}
__device__ __forceinline__ u64 get_tag(const Params& p) {
  u64 e;
  asm volatile("s_load_dwordx2 %0, %1, 0x0\n\ts_waitcnt lgkmcnt(0)" : "=s"(e) : "s"(p.ws + WS_EPOCH) : "memory");
  return tag_of(e);
}
__device__ __forceinline__ void touch(const char* a, LAS char* dump) {
  asm volatile("s_mov_b32 m0, %0\n\ts_nop 0\n\tglobal_load_lds_dword %1, off" ::"s"((unsigned)(size_t)dump), "v"(a) : "memory");
}
__device__ __forceinline__ void code_prefetch(u64 pc, LAS char* dump, int r, int lane) {
  dma16(mkd((const void*)pc, CODE_BYTES), dump, r * 1024 + lane * 16, 0);
}

constexpr int KV_IN = 18432;
struct KvPre {
  v4u m, sf;
  int sel;
  LAS char *ka[2], *va, *sa, *ta[2];
};
__device__ __forceinline__ void kv_pre(KvPre& k, LAS char* Li, LAS char* Lt, int cb, int lane) {
  const int c = lane & 15, g = lane >> 4, tok = 4 * (c & 3) + (c >> 2);
#pragma unroll
  for (int d = 0; d < 4; ++d) k.m[d] = (g & 1) == (c >> 3) && d == ((c >> 1) & 3) ? (c & 1 ? 0xffff0000u : 0xffffu) : 0u;
  k.sf = k.m & ((g >> 1) == (cb & 1) ? 0x3f803f80u : 0u);
  k.sel = g & 2 ? 0x03020302 : 0x01000100;
#pragma unroll
  for (int y = 0; y < 2; ++y) {
    const int row = 8 + tok - 2 * y - (g >> 1);
    k.ka[y] = Li + row * 128 + (((2 * cb + (g & 1)) ^ ((row >> 1) & 6)) << 4);
  }
  k.va = Li + 9216 + tok * 128 + (((4 * (cb >> 1) + g) ^ 4 ^ ((tok >> 1) & 6)) << 4);
  k.sa = Li + 17408 + cb * 128 + c * 8;
  k.ta[0] = Lt + (16 * cb + c) * 128 + (((2 * g) ^ (c & 7)) << 4);
  k.ta[1] = Lt + (16 * cb + c) * 128 + (((2 * g + 1) ^ (c & 7)) << 4);
  asm volatile("" : "+v"(k.m), "+v"(k.sf), "+v"(k.sel), "+v"(k.ka[0]), "+v"(k.ka[1]), "+v"(k.va), "+v"(k.sa), "+v"(k.ta[0]), "+v"(k.ta[1]));
}
template <int N>
__device__ __forceinline__ void kv_make(const KvPre& k) {
  v2u sk[N];
  v4u kf[N][4][2], vf[N][4];
#pragma unroll
  for (int n = 0; n < N; ++n) {
    sk[n] = *(LAS v2u*)(k.sa + n * KV_IN);
#pragma unroll
    for (int q = 0; q < 4; ++q) {
      vf[n][q] = *(LAS v4u*)(k.va + n * KV_IN + q * 2048);
#pragma unroll
      for (int y = 0; y < 2; ++y) kf[n][q][y] = *(LAS v4u*)(k.ka[y] + n * KV_IN + q * 2048);
    }
  }
  SB();
#pragma unroll
  for (int n = 0; n < N; ++n) {
    const v4u w0 = k.m & __builtin_amdgcn_perm(sk[n].x, sk[n].x, k.sel), w1 = k.m & __builtin_amdgcn_perm(sk[n].y, sk[n].y, k.sel);
    const f32x4 Z = {0.f, 0.f, 0.f, 0.f};
    v2u o2[4];
#pragma unroll
    for (int q = 0; q < 4; ++q) {
      const f32x4 o = mfma(kf[n][q][1], w1, mfma(kf[n][q][0], w0, Z)) * mfma(vf[n][q], k.sf, Z);
      o2[q] = v2u{pk(o[0], o[1]), pk(o[2], o[3])};
    }
    *(LAS v4u*)(k.ta[0] + n * 8192) = cat(o2[0], o2[1]);
    *(LAS v4u*)(k.ta[1] + n * 8192) = cat(o2[2], o2[3]);
  }
}
template <int N>
__device__ __forceinline__ void kv_store(const Params& p, LAS char* Lt, int n0, int pp, int G, int lane) {
  const int row = lane >> 3;
#pragma unroll
  for (int n = 0; n < N; ++n)
    st128<SC1>(*(LAS v4u*)(Lt + (n0 + n) * 1024 + lane * 16), mk(p.ws, WS_END),
               WS_KVT + (64 * G + 8 * (n0 + n) + row) * 4096 + 128 * pp + (((lane & 7) ^ row) << 4));
}

__device__ __forceinline__ int g2_step(int w, int k) { return k == 13 ? 26 + 27 * (w >> 1) : 27 * (w >> 1) + 13 * (w & 1) + k; }
__device__ __forceinline__ int w2_block(int k) { return (0x63052417u >> (4 * (k & 7))) & 7; }
__device__ __forceinline__ void w2_src(int (&so)[4], int lane) {
  const int row = lane >> 3;
#pragma unroll
  for (int h = 0; h < 4; ++h) so[h] = (8 * h + row) * 1280 - 1024 * h + (((lane & 7) ^ (((row >> 1) & 3) << 1)) << 4);
}
__device__ __forceinline__ void w2_fetch(v4u rw2, LAS char* L, const int (&so)[4], int w, int k, int G2) {
  dma4(rw2, L + w2_block(k) * 16384 + w * 4096, so, g2_step(w, k) * (32 * 1280) + G2 * 128);
}

__device__ __forceinline__ void stage_pads(LAS char* Lc, int lane) {
  if (lane < 16) *(LAS unsigned*)(Lc + (lane >> 3) * C_A2 + (lane & 7) * 4) = 0u;
  *(LAS v2u*)(Lc + C_B + (lane >> 4) * C_BS + (lane & 15) * 8) = v2u{0u, 0u};
}
template <int DELAYED>
__device__ __forceinline__ void stage_h(const Params& p, LAS char* Lc, int o) {
  asm volatile(
      "s_mov_b32 m0, %0\n\ts_mov_b64 exec, -2\n\tbuffer_load_dwordx4 %1, %2, 0 offen lds\n\ts_mov_b64 exec, -1\n\t"
      "buffer_load_dwordx4 %1, %2, 0 offen offset:1024 lds\n\tbuffer_load_dwordx4 %1, %2, 0 offen offset:2048 lds\n\t"
      "buffer_load_dwordx4 %1, %2, 0 offen offset:3072 lds" ::"s"((unsigned)(size_t)(Lc + DELAYED * C_A2 + 32)),
      "v"(o), "s"(mkd(p.hk - 4 * DELAYED, D * S * 2 + 4 * DELAYED))
      : "memory");
}
__device__ __forceinline__ void stage_u(const Params& p, LAS char* Lc, int ch, int lane) {
#pragma unroll
  for (int e = 0; e < 4; ++e) dma16c(mkd(p.ws, WS_END), Lc + C_B + 128 + e * C_BS, (lane >> 3) * 512 + (lane & 7) * 16, WS_KVT + ch * 4096 + e * 128);
}
__device__ __forceinline__ void stage_head(LAS char* Lc, v4u h, unsigned dbits) {
  const unsigned h01 = pk(blo(h.x) + __uint_as_float(dbits), bhi(h.x));
  *(LAS v4u*)(Lc + 32) = v4u{h01, h.y, h.z, h.w};
  *(LAS v4u*)(Lc + C_A2 + 32) = v4u{0u, h01, h.y, h.z};
}
#define OUT_AUX 0
#define TCH 1
#define RI 1
#define RJ 1
__device__ __forceinline__ int rot_of(int i, int j) { return (RI * i + RJ * j) % 10; }
__device__ __forceinline__ void kv_load(const Params& p, LAS char* L, int jb, int lw, int lane) {
  const int row = lane >> 3, o = (64 * (jb & 31) + row) * 1280 + (jb >> 5) * 128 + (((lane & 7) ^ ((row >> 1) & 2)) << 4);
  const int vo[4] = {o ^ 64, o + 9216, (o ^ 64) + 2 * 9216, o + 3 * 9216};
  const v4u rk = mkd(lw ? p.v : p.k, S * D * 2);
  LAS char* d = L + L_KV + 1024 + lw * 8192;
  dma4(rk, d, vo, 0);
  dma4(rk, d + 4096, vo, 32 * 1280);
  if (lw) dma16(mkd(p.sk, D * 8), L + L_KV + 17408, lane < 32 ? (jb >> 5) * 512 + lane * 16 : OOB, 0);
  else dma16(rk, L + L_KV, row >= 5 ? o - 8 * 1280 : OOB, 0);
}
__device__ __forceinline__ void gemm1_load(const Params& p, LAS char* L, int i, int j, int jb, int lw, int lane) {
  kv_load(p, L, jb, lw, lane);
  const v4u rx = mkd(p.x, S * D * 2), rw = mkd(p.w1, D * F * 2);
  const int i16 = lane & 15, g = lane >> 4;
  int xo[4], wo[4];
#pragma unroll
  for (int e = 0; e < 4; ++e) {
    xo[e] = (128 * i + 64 * lw + 8 * e + (lane >> 3)) * 1280 - 1024 * e + (((lane & 7) ^ (4 * (e & 1) + g)) << 4);
    const int c = i16 ^ ((g | ((e >> 1) << 2)) << 1);
    wo[e] = (32 * lw + 4 * e + g) * 3456 - 1024 * e + 256 * j + (c << 4);
    if (j == 13 && c >= 8) wo[e] = OOB;
  }
  TS(0);
  {
    const int kst = rot_of(i, j), mb = (int)(size_t)L + lw * 8192;
    int sx = kst * 128, sw = kst * (64 * 3456), sm = mb, cnt = 9, t0, t1;
#define LD4(a, b, c, d, r, s)                                                                                            \
  "buffer_load_dwordx4 %" #a ", %" #r ", %" #s " offen lds\n buffer_load_dwordx4 %" #b ", %" #r ", %" #s " offen offset:1024 lds\n" \
  "buffer_load_dwordx4 %" #c ", %" #r ", %" #s " offen offset:2048 lds\n buffer_load_dwordx4 %" #d ", %" #r ", %" #s " offen offset:3072 lds\n"
    asm volatile(
        "s_mov_b32 m0, %2\n"
        "s_branch 2f\n"
        "1:\n"
        "s_waitcnt vmcnt(16)\n"
        "s_barrier\n"
        "2:\n" LD4(8, 9, 10, 11, 16, 0)
        "s_add_u32 m0, %2, 49152\n"
        "s_add_u32 %4, %0, 32*1280\n" LD4(12, 13, 14, 15, 17, 1)
        "s_add_u32 m0, %2, 4096\n"
        "s_add_u32 %5, %1, 16*3456\n" LD4(8, 9, 10, 11, 16, 4)
        "s_add_u32 m0, %2, 49152+4096\n"
        "s_add_u32 %0, %0, 128\n" LD4(12, 13, 14, 15, 17, 5)
        "s_add_u32 %1, %1, 64*3456\n"
        "s_cmp_eq_u32 %0, 1280\n"
        "s_cselect_b32 %0, 0, %0\n"
        "s_cselect_b32 %1, 0, %1\n"
        "s_add_u32 %2, %2, 16384\n"
        "s_sub_u32 %4, %2, %18\n"
        "s_cmp_eq_u32 %4, 49152\n"
        "s_cselect_b32 %2, %18, %2\n"
        "s_mov_b32 m0, %2\n"
        "s_sub_u32 %3, %3, 1\n"
        "s_cbranch_scc0 1b\n"
        "s_waitcnt vmcnt(16)\n"
        "s_barrier\n"
        : "+&s"(sx), "+&s"(sw), "+&s"(sm), "+&s"(cnt), "=&s"(t0), "=&s"(t1)
        : "n"(0), "n"(0), "v"(xo[0]), "v"(xo[1]), "v"(xo[2]), "v"(xo[3]), "v"(wo[0]), "v"(wo[1]), "v"(wo[2]), "v"(wo[3]), "s"(rx), "s"(rw), "s"(mb)
        : "memory", "scc");
  }
}
__device__ __forceinline__ unsigned gemm1_load2(const Params& p, LAS char* L, v4u rw2, const int (&so)[4], int t0, int G2, int jb, int lw, int lane) {
  v2u tg = *(volatile LAS v2u*)(L + L_TAG);
  int ho = (2 * jb + lw) * 4096 + lane * 16;
  int xs = (16 * t0 + 48 * lw + (lane >> 3)) * 1280 + G2 * 128 + (((lane & 7) ^ (lane >> 3)) << 4);
  const v4u rx = mkd(p.x, S * D * 2);
  TS(4);
  asm volatile("s_waitcnt vmcnt(0)\n s_barrier" : "+v"(tg), "+v"(ho), "+v"(xs)::"memory");
  stage_h<1>(p, L + L_CONV + lw * CONV_BUF, ho);
#pragma unroll
  for (int h = 0; h < 6; ++h) dma16(rx, L + L_XS + (6 * lw + h) * 1024, xs, h * 8 * 1280);
  TS(5);
#pragma unroll
  for (int k = 0; k < 5; ++k) {
    w2_fetch(rw2, L, so, 2 * lw, k, G2);
    w2_fetch(rw2, L, so, 2 * lw + 1, k, G2);
  }
  TS(1);
  WAIT_V(16);
  return __builtin_amdgcn_readfirstlane(tg.y);
}

__device__ __forceinline__ unsigned gemm1_side(const Params& p, LAS char* L, int i, int j, int t0, int jb, int sw, int lane) {
  LAS char* Lc = L + L_CONV + sw * CONV_BUF;
  const int ch = 2 * jb + sw;
  if (sw) *(LAS int*)(L + L_FLAG + lane * 4) = 0;
  stage_pads(Lc, lane);
  u64 ep;
  {
    unsigned db;
    v4u hh;
    asm volatile("s_load_dwordx2 %0, %3, 0x0\n s_load_dword %1, %4, 0x0\n s_load_dwordx4 %2, %5, 0x0\n s_waitcnt lgkmcnt(0)"
                 : "=&s"(ep), "=&s"(db), "=&s"(hh)
                 : "s"(p.ws + WS_EPOCH), "s"(p.dv + jb * 4), "s"(p.hk + ch * 4096)
                 : "memory");
    stage_head(Lc, hh, sw ? db & 0xffff0000u : db << 16);
  }
  const u64 tag = tag_of(ep);
  const unsigned t32 = (unsigned)(tag >> 32);
  *(LAS v2u*)(L + L_TAG) = v2u{(unsigned)tag, t32};
  const int g = lane >> 4, q = (lane >> 2) & 3;
  const char* tch = (lane < 32 ? p.hk : p.w2 - 4096) + ch * 4096 + lane * 128;
  asm volatile("" : "+v"(tch));
  if (TCH == 0) touch(tch, L + L_DUMP);
  BAR();
  if (TCH == 3) touch(tch, L + L_DUMP);
  TS(0);
  BAR();
  TS(1);
  if (TCH == 2) touch(tch, L + L_DUMP);
  kv_store<4>(p, L + L_KT, 4 * sw, jb & 31, jb >> 5, lane);
  const bool sums = i < 10 && !(i & 1);
  f32x4 col = {0.f, 0.f, 0.f, 0.f};
#pragma unroll 1
  for (int s = 0; s < 2; ++s) {
    if (sums) {
      v4u sa[4][2];
#pragma unroll
      for (int u = 0; u < 4; ++u)
#pragma unroll
        for (int ks = 0; ks < 2; ++ks) {
          LAS char* aa = L + L_W1 + s * 16384 + (32 * ks + 8 * g + q) * 256 + (((4 * sw + u) ^ (q | ((g & 1) << 2))) << 5) + 8 * (lane & 3);
          sa[u][ks] = cat(trread(aa), trread(aa + 1024));
        }
      SB();
#pragma unroll
      for (int u = 0; u < 4; ++u) {
        const unsigned one = q == u ? 0x3f803f80u : 0u;
#pragma unroll
        for (int ks = 0; ks < 2; ++ks) col = mfma(v4u{one, one, one, one}, sa[u][ks], col);
      }
    }
    BAR();
  }
  WAIT_V(0);
  if (TCH == 1) touch(tch, L + L_DUMP);
  set_flag(p, lane < 2 ? flag_at(FK + (4 * (jb >> 5) + 2 * sw + lane) * 32 + (jb & 31)) : OOB, tag);
  __builtin_amdgcn_raw_buffer_store_b64(v2u{__float_as_uint(col[0]), t32}, mk(p.ws, WS_END),
                                        sums ? WS_CS + ((i >> 1) * 1792 + 128 * j + 64 * sw + lane) * 8 : OOB, 0, SC1);
  TS(2);
  const v4u rwd = mkd(p.ws, WS_END);
  int o0 = WS_STAT + (128 * i + 64 * sw + lane) * 8, o1 = WS_CS + (128 * j + 64 * sw + lane) * 8;
  int o2 = WS_STAT + (16 * t0 + 48 * sw + min(lane, 47)) * 8, o3 = sw ? OOB : flag_at(FK + (ch >> 4) * 32 + (lane & 31));
  int ho = jb * 8192 + lane * 16;
  LAS char *d0 = L + L_STAT + (64 * sw + lane) * 8, *d1 = L + L_CS + (64 * sw + lane) * 4;
  LAS char* d2 = L + L_STAT2 + (48 * sw + min(lane, 47)) * 8;
  asm volatile("" : "+v"(o0), "+v"(o1), "+v"(o2), "+v"(o3), "+v"(ho), "+v"(d0), "+v"(d1), "+v"(d2));
  v2u g0, g2, g1[5];
  u64 fk;
#define S_FETCH                                                                                                        \
  "buffer_load_dwordx2 %0, %10, %18, 0 offen sc1\n buffer_load_dwordx2 %1, %11, %18, 0 offen sc1\n"                    \
  "buffer_load_dwordx2 %2, %12, %18, 0 offen sc1\n buffer_load_dwordx2 %3, %13, %18, 0 offen sc1\n"                    \
  "buffer_load_dwordx2 %4, %14, %18, 0 offen sc1\n buffer_load_dwordx2 %5, %15, %18, 0 offen sc1\n"                    \
  "buffer_load_dwordx2 %6, %16, %18, 0 offen sc1\n buffer_load_dwordx2 %7, %17, %18, 0 offen sc1\n"
#define S_OUT "=&v"(g0), "=&v"(g1[0]), "=&v"(g1[1]), "=&v"(g1[2]), "=&v"(g1[3]), "=&v"(g1[4]), "=&v"(g2), "=&v"(fk)
#define S_IN "v"(o0), "v"(o1), "v"(o1 + 14336), "v"(o1 + 2 * 14336), "v"(o1 + 3 * 14336), "v"(o1 + 4 * 14336), "v"(o2), "v"(o3), "s"(rwd)
  asm volatile("s_barrier\n s_barrier\n s_barrier\n s_barrier\n s_barrier\n" S_FETCH "s_barrier\n s_waitcnt vmcnt(0)" : S_OUT : "n"(0), "n"(0), S_IN : "memory");
  const bool kv_there = sw || __builtin_amdgcn_ballot_w64(fk != tag) == 0;
  auto stage = [&]() {
    if (sw) {
      stage_h<0>(p, L + L_CONV, ho);
      stage_h<0>(p, L + L_CONV + CONV_BUF, ho + 4096);
    } else {
      stage_u(p, L + L_CONV, ch, lane);
      stage_u(p, L + L_CONV + CONV_BUF, ch + 1, lane);
    }
  };
  const bool now = kv_there && !sw;
  if (now) stage();
  TS(3);
  auto stale = [&]() {
    unsigned a = g0.y & g2.y, o = g0.y | g2.y;
#pragma unroll
    for (int n = 0; n < 5; ++n) {
      a &= g1[n].y;
      o |= g1[n].y;
    }
    return __builtin_amdgcn_ballot_w64(a != t32 || o != t32) != 0;
  };
  f32x2 st0, st2;
  float cs;
  auto digest = [&]() {
    cs = __uint_as_float(g1[0].x);
#pragma unroll
    for (int n = 1; n < 5; ++n) cs += __uint_as_float(g1[n].x);
    st0 = f32x2{blo(g0.x), __builtin_amdgcn_rcpf(bhi(g0.x))};
    st2 = f32x2{blo(g2.x), __builtin_amdgcn_rcpf(bhi(g2.x))};
  };
  const bool late = stale();
  digest();
  asm volatile("s_barrier" : "+v"(st0), "+v"(st2), "+v"(cs)::"memory");
  if (__builtin_expect(late, 0)) {
    int spin = SPIN_CAP;
#pragma unroll 1
    do asm volatile(S_FETCH "s_waitcnt vmcnt(0)" : S_OUT : "n"(0), "n"(0), S_IN : "memory");
    while (stale() && --spin);
    digest();
  }
  *(LAS f32x2*)d0 = st0;
  *(LAS float*)d1 = cs;
  lds_post2(L, F_STAT + 2 * sw);
  *(LAS f32x2*)d2 = st2;
  TS(5);
  RAW(14, __builtin_amdgcn_readfirstlane(kv_there + 1000 * late));
  if (!now) {
    if (!kv_there) wait_flag<2>(p, o3, tag);
    stage();
  }
  asm volatile("s_waitcnt vmcnt(0) lgkmcnt(0)" ::: "memory");
  TS(6);
  return t32;
}

struct ConvPre {
  v4u ba;
  int aa, fl, lb, cw, kw, yr, yo;
};
__device__ __forceinline__ void conv_pre(ConvPre& c, LAS char* L, LAS char* Lc, int ch, int me, int lane) {
  const int pw = me & 1, col = lane & 15, g = lane >> 4, b = (int)(size_t)Lc;
  c.aa = b + pw * C_A2 + 32 + 64 * g - 8 * ((lane >> 2) & 3) + 8 * (lane & 3);
#pragma unroll
  for (int e = 0; e < 4; ++e) {
    const int u = 7 + col - 2 * g + 8 * (e >> 1) + (e & 1);
    c.ba[e] = b + C_B + pw * 64 + ((u >> 2) & 3) * C_BS + (u >> 4) * 128 + (u & 3) * 8;
  }
  c.fl = (int)(size_t)L + L_FLAG + (F_PAIR + me) * 4;
  c.lb = b + lane * 16;
  const int Q = (4 * col + g + 1) & 63;
  c.cw = b + C_B + pw * 2304 + (Q & 3) * 256 + (Q >> 2) * 16;
  c.kw = b + 2048 + pw * 2048 + 32 * col + 8 * g;
  c.yr = b + 2048 + (11 * pw + g) * 192 + 8 * col;
  c.yo = WS_YT + ch * YS + pw * 11 * 256 + lane * 16;
  asm volatile("" : "+v"(c.ba), "+v"(c.aa), "+v"(c.fl), "+v"(c.lb), "+v"(c.cw), "+v"(c.kw), "+v"(c.yr), "+v"(c.yo));
}
template <typename T>
__device__ __forceinline__ LAS T& at(int a) { return *(LAS T*)(size_t)a; }
template <int H>
__device__ __forceinline__ void conv_send(const ConvPre& cp, const f32x32& z0) {
#pragma unroll
  for (int c = 4 * H; c < 4 * H + 4; ++c) at<f32x4>(cp.lb + c * 1024) = f32x4{z0[4 * c], z0[4 * c + 1], z0[4 * c + 2], z0[4 * c + 3]};
  asm volatile("; half %0" ::"n"(H) : "memory");
}
template <int H>
__device__ __forceinline__ void conv_keep(const ConvPre& cp, const f32x32& z0) {
  f32x4 y[4];
  const f32x4 c0 = at<f32x4>(cp.lb + C_B + H * 1024), c1 = at<f32x4>(cp.lb + C_B + 2304 + H * 1024);
#pragma unroll
  for (int c = 0; c < 4; ++c) y[c] = at<f32x4>(cp.lb + (4 * H + c) * 1024);
  SB();
#pragma unroll
  for (int c = 0; c < 4; ++c) {
    const int b = 16 * H + 4 * c;
    at<v2u>(cp.kw + 512 * c) = v2u{pks(y[c][0] + z0[b] + c0[c] + c1[c], y[c][1] + z0[b + 1]), pks(y[c][2] + z0[b + 2], y[c][3] + z0[b + 3])};
  }
  asm volatile("; half %0" ::"n"(H) : "memory");
}
__device__ __forceinline__ void pair_sync(const ConvPre& cp, int val) {
  asm volatile("" ::: "memory");
  at<volatile int>(cp.fl) = val;
  lds_wait_at(cp.fl ^ 4, val);
}
__device__ __forceinline__ void conv(const Params& p, const ConvPre& cp, int me, int round, bool g1, v4u rw2, int so, int s12, int s13, u32x32& held,
                                     unsigned t32) {
  const int pw = me & 1;
  u32x32 a0, a1, b0, b1;
  asm volatile(
      ".set n, 0\n"
      ".rept 16\n"
      " ds_read_b64_tr_b16 v[4*n:4*n+1], %4 offset:256*n+32\n"
      " ds_read_b64_tr_b16 v[4*n+2:4*n+3], %4 offset:256*n\n"
      " ds_read_b64 v[64+4*n:65+4*n], v[192+2*(n&1)] offset:32+128*(n/2)\n"
      " ds_read_b64 v[66+4*n:67+4*n], v[193+2*(n&1)] offset:32+128*(n/2)\n"
      " .set n, n+1\n"
      ".endr\n"
      : "=&{v[0:31]}"(a0), "=&{v[32:63]}"(a1), "=&{v[64:95]}"(b0), "=&{v[96:127]}"(b1)
      : "v"(cp.aa), "{v[192:195]}"(cp.ba)
      : "memory");
  if (__builtin_expect(g1, 1)) {
    asm volatile(
        ".set n, 0\n .rept 4\n buffer_load_dwordx4 v[224+4*n:227+4*n], %1, %2, %3 offen\n buffer_load_dwordx4 v[240+4*n:243+4*n], %1, %2, %4 offen\n"
        " .if n < 3\n v_add_u32 %1, 8*1280, %1\n .endif\n .set n, n+1\n .endr\n"
        "s_waitcnt vmcnt(8) lgkmcnt(0)\n"
        : "=&{v[224:255]}"(held), "+v"(so)
        : "s"(rw2), "s"(s12), "s"(s13)
        : "memory");
    ST(8);
    asm volatile("s_barrier" ::: "memory");
  } else if (me >= 4) {
    int a = cp.fl;
    asm volatile("" : "+v"(a));
    lds_wait_at<2>(a - 16, 2);
  }
  ST(9);
  f32x32 z1, z0;
  asm volatile(
      "s_waitcnt lgkmcnt(0)\n"
      "s_setprio 1\n"
      ".set m, 0\n"
      ".set r, 0\n"
      ".set y, 0\n"
      ".rept 2\n"
      " .set c, 7\n"
      " .rept 8\n"
      "  .set n, 0\n"
      "  .rept 2*c+2\n"
      "   .if m == 72\n"
      "    s_waitcnt lgkmcnt(4)\n"
      "   .endif\n"
      "   .if m == 84\n"
      "    s_waitcnt lgkmcnt(0)\n"
      "    ds_write_b32 %8, %9\n"
      "   .endif\n"
      "   .set z, 128+32*(m/72)+4*c\n"
      "   .set b, 64+4*(2*c+1-n)\n"
      "   .if (m < 72) && (n == 0)\n"
      "    v_mfma_f32_16x16x32_bf16 v[z:z+3], v[0:3], v[b:b+3], 0\n"
      "   .else\n"
      "    v_mfma_f32_16x16x32_bf16 v[z:z+3], v[4*n:4*n+3], v[b:b+3], v[z:z+3]\n"
      "   .endif\n"
      "   .set cr, 7-r/4\n"
      "   .set cy, 7-y/4\n"
      "   .if (r < 32) && ((m >= 72) || (c < cr) || ((c == cr) && (n >= 2)))\n"
      "    .set b, 2*cr+1-(r&3)/2\n"
      "    ds_read_b64 v[64+4*b+2*(r&1):65+4*b+2*(r&1)], v[192+2*(b&1)+(r&1)] offset:128*(b/2)\n"
      "    .set r, r+1\n"
      "   .elseif (y < 32) && (m >= 80-cy*(cy+1))\n"
      "    .if y&3\n"
      "     v_mov_b32 v[160+4*cy+(y&3)], v[127+4*cy+(y&3)]\n"
      "    .else\n"
      "     v_mov_b32 v[160+4*cy], 0\n"
      "    .endif\n"
      "    .set y, y+1\n"
      "   .endif\n"
      "   .set m, m+1\n"
      "   .set n, n+1\n"
      "  .endr\n"
      "  .set c, c-1\n"
      " .endr\n"
      ".endr\n"
      "s_setprio 3\n"
      "s_waitcnt vmcnt(0)\n"
      "s_nop 5\n"
      : "=&{v[128:159]}"(z1), "=&{v[160:191]}"(z0), "+{v[64:95]}"(b0), "+{v[96:127]}"(b1), "+{v[224:255]}"(held)
      : "{v[0:31]}"(a0), "{v[32:63]}"(a1), "{v[192:195]}"(cp.ba), "v"(cp.fl), "v"(2 + 8 * round)
      : "memory");
  ST(10);
  const int lane = lane_id();
  lds_wait_at(cp.fl ^ 4, 2 + 8 * round);
  {
    const bool last = lane == 63;
    at<f32x4>(cp.cw) = f32x4{last ? 0.f : z1[3], last ? z1[3] : z1[7], last ? z1[7] : z1[11], last ? z1[11] : z1[15]};
    at<f32x4>(cp.cw + 1024) = f32x4{last ? z1[15] : z1[19], last ? z1[19] : z1[23], last ? z1[23] : z1[27], last ? z1[27] : z1[31]};
  }
  if (pw) conv_send<0>(cp, z0);
  else conv_send<1>(cp, z0);
  pair_sync(cp, 3 + 8 * round);
  if (pw) conv_keep<1>(cp, z0);
  else conv_keep<0>(cp, z0);
  int r2 = cp.yr - (lane & (32 * pw)) * 4, o2 = lane < 48 ? cp.yo + 2048 : OOB;
  asm volatile("" : "+v"(r2), "+v"(o2));
  pair_sync(cp, 4 + 8 * round);
  v2u ya[3], yb[3];
#pragma unroll
  for (int n = 0; n < 3; ++n) {
    LAS char* a = (LAS char*)(size_t)(n < 2 ? cp.yr : r2) + n * 768;
    ya[n] = trread(a);
    yb[n] = trread(a + 128);
  }
  SB();
#pragma unroll
  for (int n = 0; n < 3; ++n) st128<SC1>(v4u{ya[n].x, ya[n].y, yb[n].x, t32}, mk(p.ws, WS_END), n < 2 ? cp.yo + n * 1024 : o2);
  ST(12);
}

__device__ __forceinline__ int xs_at(int a, int lane) { return (lane & 15) * 128 + (((2 * a + (lane >> 5)) ^ (lane & 7)) << 4) + ((lane >> 4) & 1) * 8; }
#define Y_NAP 8
__device__ __forceinline__ void finish(const Params& p, LAS char* L, const v2u (&xv)[NT], int t0, int rng, int G2, int a, unsigned t32, int lane) {
  const int i16 = lane & 15, g = lane >> 4;
  float x[NT][4], s[NT][4];
#pragma unroll
  for (int u = 0; u < NT; ++u) {
    const f32x2 st = *(LAS f32x2*)(L + L_STAT2 + (16 * u + i16) * 8);
    x[u][0] = blo(xv[u].x), x[u][1] = bhi(xv[u].x), x[u][2] = blo(xv[u].y), x[u][3] = bhi(xv[u].y);
#pragma unroll
    for (int r = 0; r < 4; ++r) {
      s[u][r] = (x[u][r] - st.x) * st.y;
      asm volatile("" : "+v"(s[u][r]), "+v"(x[u][r]));
    }
  }
  const v2u bv = *(LAS v2u*)(L + L_B2 + a * 32 + g * 8);
  float b2[4] = {blo(bv.x), bhi(bv.x), blo(bv.y), bhi(bv.y)};
  int yo = (4 * g * YS + i16 * 16), za = (int)(size_t)L + a * 1024 + lane * 16, zb = za + 65536, fa = (int)(size_t)L + L_FLAG + (F_DONE + (lane & 3)) * 4;
  int oo = (16 * t0 + i16) * 1280 + G2 * 128 + a * 32 + g * 8;
  asm volatile("" : "+v"(b2[0]), "+v"(b2[1]), "+v"(b2[2]), "+v"(b2[3]), "+v"(yo), "+v"(za), "+v"(zb), "+v"(fa), "+v"(oo));
  SB();
  ST(13);
  i32x16 yy;
  {
    u64 m0 = -1, m1 = -1, m2 = -1, m3 = -1;
    int cnt = SPIN_CAP;
    const int yb = WS_YT + (64 * G2 + 16 * a) * YS + rng * 256;
    asm volatile(
        "s_branch 2f\n"
        "1:\n"
        "s_sleep %13\n"
        "2:\n"
        "s_mov_b64 exec, %1\n buffer_load_dwordx4 v[0:3], %6, %7, %8 offen sc1\n"
        "s_mov_b64 exec, %2\n buffer_load_dwordx4 v[4:7], %6, %7, %9 offen sc1\n"
        "s_mov_b64 exec, %3\n buffer_load_dwordx4 v[8:11], %6, %7, %10 offen sc1\n"
        "s_mov_b64 exec, %4\n buffer_load_dwordx4 v[12:15], %6, %7, %11 offen sc1\n"
        "s_mov_b64 exec, -1\n"
        "s_sub_u32 %5, %5, 1\n"
        "s_waitcnt vmcnt(0)\n"
        "s_cbranch_scc1 3f\n"
        "v_cmp_ne_u32 %1, %12, v3\n v_cmp_ne_u32 %2, %12, v7\n v_cmp_ne_u32 %3, %12, v11\n v_cmp_ne_u32 %4, %12, v15\n"
        "s_or_b64 vcc, %1, %2\n s_or_b64 vcc, vcc, %3\n s_or_b64 vcc, vcc, %4\n"
        "s_cbranch_scc1 1b\n"
        "3:\n"
        : "=&{v[0:15]}"(yy), "+&s"(m0), "+&s"(m1), "+&s"(m2), "+&s"(m3), "+&s"(cnt)
        : "v"(yo), "s"(mkd(p.ws, WS_END)), "s"(yb), "s"(yb + YS), "s"(yb + 2 * YS), "s"(yb + 3 * YS), "s"(t32), "n"(Y_NAP)
        : "memory", "vcc", "scc");
  }
  ST(11);
#pragma unroll
  for (int u = 0; u < NT; ++u)
#pragma unroll
    for (int r = 0; r < 4; ++r) {
      s[u][r] *= u & 1 ? bhi(yy[4 * r + (u >> 1)]) : blo(yy[4 * r + (u >> 1)]);
      asm volatile("" : "+v"(s[u][r]));
    }
  SB();
  ST(5);
#pragma unroll
  for (int P = 0; P < 3; ++P) {
    if (P == 0) lds_wait_at<1, 0>(fa, 1);
    if (P == 1) lds_wait_at<0, 16>(fa, 1);
    if (P == 2) lds_wait_at<0, 32>(fa, 1);
    f32x4 z[2][4];
#pragma unroll
    for (int e = 0; e < 2; ++e)
#pragma unroll
      for (int w = 0; w < 4; ++w) z[e][w] = at<f32x4>((P < 2 ? za + P * 32768 : zb) + (e ^ (w & 1)) * 16384 + w * 4096);
    SB();
#pragma unroll
    for (int e = 0; e < 2; ++e) {
      float o[4];
#pragma unroll
      for (int r = 0; r < 4; ++r) {
        float t = z[e][0][r] + b2[r], v = z[e][1][r] + z[e][2][r];
        asm("" : "+v"(t), "+v"(v));
        t += z[e][3][r];
        asm("" : "+v"(t));
        t += v;
        asm("" : "+v"(t));
        float gt = t * __builtin_amdgcn_rcpf(1.f + __builtin_amdgcn_exp2f(t * -1.442695041f));
        asm("" : "+v"(gt));
        o[r] = fmaf(s[2 * P + e][r], gt, x[2 * P + e][r]);
      }
      __builtin_amdgcn_raw_buffer_store_b64(v2u{pks(o[0], o[1]), pks(o[2], o[3])}, mk(p.out, S * D * 2), oo, (2 * P + e) * 16 * 1280, OUT_AUX);
    }
    if (P == 1) ST(6);
  }
  ST(7);
}

#define ASM_TS(n)
#define ASM_TC(n)
#define B9 "3"
#define HB "5"
#define HN "11"
#define V10(a) "v" #a "0", "v" #a "1", "v" #a "2", "v" #a "3", "v" #a "4", "v" #a "5", "v" #a "6", "v" #a "7", "v" #a "8", "v" #a "9"
__device__ __forceinline__ void mma_waves(const Params& p, LAS char* L, int i, int j, bool job, int t0, int G2, int w, int lane) {
  const int fh = w & 1, th = w >> 1, i16 = lane & 15, g = lane >> 4, q = (lane >> 2) & 3, Lb = (int)(size_t)L;
  const int fb = 128 * j + 64 * fh;
  v2u b1v[4];
#pragma unroll
  for (int m = 0; m < 4; ++m) b1v[m] = __builtin_amdgcn_raw_buffer_load_b64(mk(p.b1, F * 2), (fb + 16 * m + 4 * g) * 2, 0, 0);
  const v2u b2v = __builtin_amdgcn_raw_buffer_load_b64(mk(p.b2, D * 2), (64 * G2 + 16 * w + 4 * g) * 2, 0, 0);
  u64 ep = ask_epoch(p);
  SB();
  const int sb = 27 * th + 13 * fh, ss = 26 + 27 * th;
  i32x16 va, vb;
#pragma unroll
  for (int m = 0; m < 4; ++m) va[m] = Lb + L_W1 + (8 * g + q) * 256 + (((4 * fh + m) ^ (q | ((g & 1) << 2))) << 5) + 8 * (lane & 3);
#pragma unroll
  for (int ks = 0; ks < 2; ++ks) va[4 + ks] = Lb + (64 * th + i16) * 128 + (((4 * ks + g) ^ (i16 >> 1)) << 4);
  va[6] = Lb + L_STAT + (64 * th + i16) * 8;
  va[7] = Lb + L_CS + (64 * fh + 4 * g) * 4;
  va[8] = Lb + L_FLAG + w * 4;
  va[9] = Lb + L_FLAG + (lane & 3) * 4;
  va[10] = lane * 16 + WS_HT + (4 * j + 2 * fh) * HS + (8 * i + 4 * th) * 1024;
  va[11] = va[10] + HS;
  va[14] = 1;
  {
    int so[4];
    w2_src(so, lane);
    va[15] = so[0];
  }
  {
    const int rr = 4 * g + q;
#pragma unroll
    for (int dd = 0; dd < 4; ++dd) {
      vb[dd] = Lb + w * 4096 + rr * 128 + ((dd ^ ((rr >> 1) & 3)) << 5) + 8 * (lane & 3);
      vb[4 + dd] = vb[dd] + 65536;
    }
    vb[8] = lane * 16 + fh * 1024;
    vb[9] = lane * 16 + 1024 - fh * 1024;
    vb[10] = vb[8] + 4096;
    vb[11] = vb[9] + 4096;
    vb[12] = Lb + w * 4096 + lane * 16;
    vb[13] = vb[12] + 65536;
    vb[14] = vb[15] = va[8];
    va[12] = lane == 0 ? flag_at(FH + (2 * i + th) * 32 + 2 * j + fh) : OOB;
    const int tl = min(t0 + 4 * (lane >> 3), t0 + NT - 1), c = lane & 7;
    va[13] = flag_at(FH + (tl >> 2) * 32 + (c == 7 ? ss >> 1 : (sb >> 1) + c));
  }
  f32x32 acc[2];
  asm volatile(
      ".set n, 0\n"
      ".rept 32\n"
      " v_mov_b64 v[n:n+1], 0\n"
      " .set n, n+2\n"
      ".endr\n"
      : "={v[0:31]}"(acc[0]), "={v[32:63]}"(acc[1]), "+{v[128:143]}"(va));
  int sh = WS_HT + t0 * 1024 + sb * HS, hs = WS_HT + t0 * 1024 + ss * HS;
  int role = __builtin_amdgcn_readfirstlane((fb < F ? 1 : 0) + (job ? 2 : 0));
  int sw = G2 * 128 + (sb + 7) * 40960;
  asm volatile("" : "+s"(sh), "+s"(hs), "+s"(role), "+s"(sw));
  KvPre kp;
  kv_pre(kp, L + L_KV, L + L_KT, w, lane);
  BAR();
  asm volatile("" : "+s"(ep));
  kv_make<1>(kp);
  SB();
  f32x16 bb;
#pragma unroll
  for (int m = 0; m < 4; ++m) {
    bb[4 * m] = blo(b1v[m].x);
    bb[4 * m + 1] = bhi(b1v[m].x);
    bb[4 * m + 2] = blo(b1v[m].y);
    bb[4 * m + 3] = bhi(b1v[m].y);
  }
  *(LAS v2u*)(L + L_B2 + w * 32 + g * 8) = b2v;
  {
    int cnt, s12;
    asm volatile(
        ".macro G1RD n, s, sl, ks\n"
        " .if (\\n %% 3) == 2\n"
        "  ds_read_b128 v[\\s+16+4*(\\n/3):\\s+19+4*(\\n/3)], v[132+\\ks] offset:\\sl*16384+(\\n/3)*2048\n"
        " .else\n"
        "  ds_read_b64_tr_b16 v[\\s+4*(\\n/3)+2*(\\n%%3):\\s+4*(\\n/3)+2*(\\n%%3)+1], v[128+\\n/3] offset:\\sl*16384+\\ks*8192+(\\n%%3)*1024\n"
        " .endif\n"
        ".endm\n"
        ".macro STRD n\n"
        " .if \\n == 0\n"
        "  ds_read_b32 v164, v137 offset:%c12*4\n"
        " .elseif \\n < 5\n"
        "  ds_read_b64 v[166+2*\\n:167+2*\\n], v134 offset:(\\n-1)*128\n"
        " .else\n"
        "  ds_read_b128 v[156+4*\\n:159+4*\\n], v135 offset:(\\n-5)*64\n"
        " .endif\n"
        ".endm\n"
        ".macro G1H c, s, sl, ks, st\n"
        " .set n, 0\n"
        " .rept 16\n"
        "  v_mfma_f32_16x16x32_bf16 v[4*n:4*n+3], v[\\c+4*(n&3):\\c+4*(n&3)+3], v[\\c+16+4*(n/4):\\c+19+4*(n/4)], v[4*n:4*n+3]\n"
        "  .if (n < 12) && (\\sl >= 0)\n"
        "   G1RD n, \\s, \\sl, \\ks\n"
        "  .endif\n"
        "  .if (n >= 4) && (n < 13) && \\st\n"
        "   .set m, n-4\n"
        "   STRD m\n"
        "  .endif\n"
        "  .set n, n+1\n"
        " .endr\n"
        ".endm\n"
        ".macro LWAIT a, f, v\n"
        " s_mov_b32 %7, 0x400000\n"
        " 8:\n"
        " ds_read_b32 v255, \\a offset:\\f*4\n"
        " s_sub_u32 %7, %7, 1\n"
        " s_cbranch_scc1 9f\n"
        " s_waitcnt lgkmcnt(0)\n"
        " v_cmp_gt_i32 vcc, \\v, v255\n"
        " s_cbranch_vccnz 8b\n"
        " 9:\n"
        ".endm\n"
        ".macro PK4 op, d, a, b, c, da, db, dc, mod:vararg\n"
        " .set e, 0\n"
        " .rept 4\n"
        "  .if \\c >= 0\n"
        "   \\op v[\\d+e:\\d+e+1], v[\\a+\\da*e:\\a+\\da*e+1], v[\\b+\\db*e:\\b+\\db*e+1], v[\\c+\\dc*e:\\c+\\dc*e+1] \\mod\n"
        "  .else\n"
        "   \\op v[\\d+e:\\d+e+1], v[\\a+\\da*e:\\a+\\da*e+1], v[\\b+\\db*e:\\b+\\db*e+1] \\mod\n"
        "  .endif\n"
        "  .set e, e+2\n"
        " .endr\n"
        ".endm\n"
        ".macro TR8 op\n"
        " .set e, 0\n"
        " .rept 8\n"
        "  \\op v[64+e], v[64+e]\n"
        "  .set e, e+1\n"
        " .endr\n"
        ".endm\n"
        ".macro HST y\n"
        " buffer_store_dwordx4 v[16*(\\y&3)+8*(\\y/4):16*(\\y&3)+8*(\\y/4)+3], v[138+\\y/4], %8, 0 offen offset:(\\y&3)*1024 sc1\n"
        ".endm\n"
        ".macro HSL n\n"
        " .if (y >= " HB ") && (yp < y) && (" HN " & \\n)\n"
        "  HST yp\n"
        "  .set yp, yp+1\n"
        " .endif\n"
        ".endm\n"
        ".macro PUBLISH n\n"
        " s_waitcnt vmcnt(\\n)\n"
        " buffer_store_dwordx2 v[68:69], v140, %8, 0 offen sc1\n"
        ".endm\n"
        ".macro G2RD k, n\n"
        " .set b, (0x63052417 >> (4*(\\k&7))) & 7\n"
        " .set ws, 144+16*((\\k+3)%%6)\n"
        " ds_read_b64_tr_b16 v[ws+2*\\n:ws+2*\\n+1], v[240+4*(b/4)+\\n/2] offset:(b&3)*16384+(\\n&1)*2048\n"
        ".endm\n"
        ".macro G2RDS q0, cnt\n"
        " .set rq, \\q0\n"
        " .rept \\cnt\n"
        "  .set rk, rq/8\n"
        "  .set rn, rq&7\n"
        "  G2RD rk, rn\n"
        "  .set rq, rq+1\n"
        " .endr\n"
        ".endm\n"
        ".macro W2LD k\n"
        " s_add_u32 m0, %19, ((0x63052417 >> (4*(\\k&7))) & 7)*16384\n"
        " s_add_u32 %7, %18, (\\k-7)*40960\n"
        " buffer_load_dwordx4 v143, %17, %7 offen lds\n"
        " buffer_load_dwordx4 v72, %17, %7 offen offset:1024 lds\n"
        " buffer_load_dwordx4 v73, %17, %7 offen offset:2048 lds\n"
        " buffer_load_dwordx4 v74, %17, %7 offen offset:3072 lds\n"
        ".endm\n"
        ".macro G2AT q\n"
        " .if \\q < 66\n"
        "  .set gk, \\q / 6\n"
        "  .set gu, \\q %% 6\n"
        " .else\n"
        "  .set gu, 2*((\\q-66)/5)\n"
        "  .set gk, 11+(\\q-66)%%5\n"
        "  .if gk > 12\n"
        "   .set gu, gu+1\n"
        "   .set gk, gk-2\n"
        "  .endif\n"
        " .endif\n"
        ".endm\n"
        ".macro G2LD q\n"
        " G2AT \\q\n"
        " .if gk < 12\n"
        "  buffer_load_dwordx4 v[96+4*(\\q%%12):99+4*(\\q%%12)], v[248+(gu&1)+2*(gu/4)], %8, %6 offen offset:(gu&2)*1024 sc1\n"
        " .elseif gk == 12\n"
        "  buffer_load_dwordx4 v[96+4*(\\q%%12):99+4*(\\q%%12)], v[248+(gu&1)+2*(gu/4)], %8, %2 offen offset:(gu&2)*1024 sc1\n"
        " .else\n"
        "  buffer_load_dwordx4 v[96+4*(\\q%%12):99+4*(\\q%%12)], v[248+(gu&1)+2*(gu/4)], %8, %9 offen offset:(gu&2)*1024 sc1\n"
        " .endif\n"
        ".endm\n"
        ".macro G2MF d\n"
        " .set ws, 144+16*((k+3)%%6)+4*\\d\n"
        " .if k > 0\n"
        "  v_mfma_f32_16x16x32_bf16 v[16*u+4*\\d:16*u+4*\\d+3], v[ws:ws+3], v[96+4*(q%%12):99+4*(q%%12)], v[16*u+4*\\d:16*u+4*\\d+3]\n"
        " .else\n"
        "  v_mfma_f32_16x16x32_bf16 v[16*u+4*\\d:16*u+4*\\d+3], v[ws:ws+3], v[96+4*q:99+4*q], 0\n"
        " .endif\n"
        ".endm\n"
        ".macro G2WR t, d\n"
        " ds_write_b128 v[252+\\t/4], v[16*\\t+4*\\d:16*\\t+4*\\d+3] offset:(\\t&3)*16384+\\d*1024\n"
        " .if (\\t&1) && (\\d == 3)\n"
        "  ds_write_b32 v254, v254 offset:(%c14+4*(\\t/2))*4\n"
        " .endif\n"
        ".endm\n"
        "s_branch 20f\n"
        "3:\n"
        "LWAIT v137, %c12, 1\n"
        ".set m, 1\n"
        ".rept 8\n"
        " STRD m\n"
        " .set m, m+1\n"
        ".endr\n"
        "s_waitcnt lgkmcnt(0)\n"
        "s_branch 4f\n"
        "5:\n"
        "s_barrier\n"
        "G2RDS 0, 24\n"
        "s_branch 6f\n"
        "10:\n"
        "PUBLISH 0\n"
        "s_branch 7f\n"
        "11:\n"
        "LWAIT v254, 4, 7\n"
        "s_branch 12f\n"
        "20:\n"
        "v_mov_b32 v160, 0xbdd2d3e8\n"
        "v_mov_b32 v161, 0xc0135761\n"
        "v_mov_b32 v162, 1.0\n"
        "s_mov_b32 %7, 2\n"
        "s_waitcnt lgkmcnt(0)\n"
        "s_barrier\n" ASM_TS(0) ASM_TC(8)
        ".set n, 0\n"
        ".rept 12\n"
        " G1RD n, 64, 0, 0\n"
        " .set n, n+1\n"
        ".endr\n"
        "1:\n"
        "s_waitcnt lgkmcnt(0)\n"
        "G1H 64, 96, 0, 1, 0\n"
        "s_waitcnt lgkmcnt(0)\n"
        "s_barrier\n"
        "G1H 96, 64, 1, 0, 0\n"
        "s_waitcnt lgkmcnt(0)\n"
        "G1H 64, 96, 1, 1, 0\n"
        "s_waitcnt lgkmcnt(0)\n"
        "s_barrier\n"
        "G1H 96, 64, 2, 0, 0\n"
        "s_waitcnt lgkmcnt(0)\n"
        "G1H 64, 96, 2, 1, 0\n"
        "s_waitcnt lgkmcnt(0)\n"
        "s_barrier\n"
        "s_sub_u32 %7, %7, 1\n"
        "G1H 96, 64, 0, 0, 0\n"
        "s_cbranch_scc0 1b\n"
        "s_waitcnt lgkmcnt(0)\n"
        "G1H 64, 96, 0, 1, 1\n"
        "s_waitcnt lgkmcnt(0)\n"
        "v_cmp_eq_u32 vcc, 0, v164\n"
        "s_bitcmp1_b32 %11, 0\n"
        "G1H 96, 64, -1, 0, 0\n"
        "s_setprio 2\n" ASM_TS(1) ASM_TC(9)
        "s_cbranch_scc0 5b\n"
        "s_cbranch_vccnz 3b\n"
        "4:\n" ASM_TS(2)
        ".set y, 0\n"
        ".set yp, 0\n"
        ".rept 8\n"
        " .set a, 16*(y&3)+8*(y/4)\n"
        " .set t, 168+2*(y&3)\n"
        " PK4 v_pk_fma_f32, a, t, 176+8*(y/4), a, 0, 1, 1, op_sel_hi:[0,1,1] neg_lo:[1,0,0] neg_hi:[1,0,0]\n"
        " HSL 1\n"
        " PK4 v_pk_fma_f32, a, a, t, 144+8*(y/4), 1, 0, 1, op_sel:[0,1,0]\n"
        " .if y == " B9 "\n"
        "  s_barrier\n"
        " .endif\n"
        " PK4 v_pk_mul_f32, 64, a, a, -1, 1, 1, 0\n"
        " PK4 v_pk_fma_f32, 64, 64, 160, 160, 1, 0, 0, op_sel:[0,0,1] op_sel_hi:[1,0,1]\n"
        " PK4 v_pk_mul_f32, 64, a, 64, -1, 1, 1, 0\n"
        " HSL 2\n"
        " TR8 v_exp_f32\n"
        " HSL 4\n"
        " .if (y >= " B9 ") && (y < " B9 "+3)\n"
        "  .set g, 8*(y-" B9 ")\n"
        "  G2RDS g, 4\n"
        " .endif\n"
        " PK4 v_pk_add_f32, 64, 64, 162, -1, 1, 0, 0, op_sel_hi:[1,0]\n"
        " HSL 8\n"
        " TR8 v_rcp_f32\n"
        " HSL 16\n"
        " .if (y >= " B9 ") && (y < " B9 "+3)\n"
        "  .set g, 8*(y-" B9 ")+4\n"
        "  G2RDS g, 4\n"
        " .endif\n"
        " PK4 v_pk_mul_f32, a, a, 64, -1, 1, 1, 0\n"
        " v_cvt_pk_bf16_f32 v[a], v[a], v[a+1]\n"
        " v_cvt_pk_bf16_f32 v[a+1], v[a+2], v[a+3]\n"
        " v_cvt_pk_bf16_f32 v[a+2], v[a+4], v[a+5]\n"
        " v_cvt_pk_bf16_f32 v[a+3], v[a+6], v[a+7]\n"
        " .set y, y+1\n"
        ".endr\n"
        ".rept 8-yp\n"
        " HST yp\n"
        " .set yp, yp+1\n"
        ".endr\n"
        "6:\n" ASM_TS(3)
        "v_mov_b64 v[68:69], %10\n"
        "s_bitcmp1_b32 %11, 1\n"
        "v_add_u32 v72, 9216, v143\n"
        "v_add_u32 v73, 2*9216, v143\n"
        "v_add_u32 v74, 3*9216, v143\n"
        "s_waitcnt lgkmcnt(0)\n"
        "s_barrier\n"
        "s_cbranch_scc0 10b\n"
        "G2RDS 24, 24\n"
        "W2LD 8\n"
        "W2LD 9\n"
        "PUBLISH 8\n" ASM_TS(4)
        "W2LD 10\n"
        "W2LD 7\n"
        "s_waitcnt lgkmcnt(0)\n"
        "ds_write_b32 v136, v142 offset:%c13*4\n" ASM_TS(5)
        "W2LD 11\n"
        "s_add_u32 %2, %6, 12*%c16\n"
        "s_mov_b32 %7, 0x8000\n"
        "8:\n"
        "buffer_load_dwordx2 v[70:71], v141, %8, 0 offen sc1\n"
        "s_sub_u32 %7, %7, 1\n"
        "s_cbranch_scc1 9f\n"
        "s_waitcnt vmcnt(0)\n"
        "v_cmp_ne_u64 vcc, %10, v[70:71]\n"
        "s_mov_b64 exec, vcc\n"
        "s_cbranch_vccnz 8b\n"
        "9:\n"
        "s_mov_b64 exec, -1\n" ASM_TS(6)
        ".set q, 0\n"
        ".rept 11\n"
        " G2LD q\n"
        " .if q == 5\n"
        "  s_add_u32 %6, %6, %c16\n"
        " .endif\n"
        " .set q, q+1\n"
        ".endr\n"
        "s_setprio 0\n"
        ".set q, 0\n"
        ".rept 81\n"
        " G2AT q\n"
        " .set k, gk\n"
        " .set u, gu\n"
        " .set o, 10\n"
        " .if q > 70\n"
        "  .set o, 80-q\n"
        " .endif\n"
        " .if ((q%%6) == 0) && (q >= 36) && (q <= 66)\n"
        "  s_waitcnt vmcnt(o) lgkmcnt(0)\n"
        " .else\n"
        "  s_waitcnt vmcnt(o)\n"
        " .endif\n"
        " G2MF 0\n"
        " .set ql, q+11\n"
        " .if ql < 81\n"
        "  G2LD ql\n"
        " .endif\n"
        " G2MF 1\n"
        " .if (u == 0) && (ql < 66)\n"
        "  s_add_u32 %6, %6, %c16\n"
        " .endif\n"
        " .if q == 53\n"
        "  ds_read_b32 v255, v254 offset:16\n"
        " .endif\n"
        " .if q == 54\n"
        "  v_cmp_gt_i32 vcc, 7, v255\n"
        " .endif\n"
        " .set t, u-1\n"
        " .set dw, 2*(k-11)\n"
        " .if (k >= 11) && (k <= 12) && (u > 0)\n"
        "  G2WR t, dw\n"
        " .endif\n"
        " G2MF 2\n"
        " .set kr, k+3\n"
        " .if (u >= 1) && (u <= 4) && (k >= 3) && (k <= 10)\n"
        "  .set n, 2*u-2\n"
        "  G2RD kr, n\n"
        " .endif\n"
        " G2MF 3\n"
        " .if (u >= 1) && (u <= 4) && (k >= 3) && (k <= 10)\n"
        "  .set n, 2*u-1\n"
        "  G2RD kr, n\n"
        " .endif\n"
        " .set dw, dw+1\n"
        " .if (k >= 11) && (k <= 12) && (u > 0)\n"
        "  G2WR t, dw\n"
        " .endif\n"
        " .if q == 54\n"
        "  s_cbranch_vccnz 11b\n"
        "  12:\n"
        " .endif\n"
        " .set q, q+1\n"
        ".endr\n"
        "s_nop 4\n"
        "G2WR 5, 0\n"
        "G2WR 5, 1\n"
        "G2WR 5, 2\n"
        "G2WR 5, 3\n"
        "7:\n"
        ".purgem G1RD\n .purgem STRD\n .purgem G1H\n .purgem LWAIT\n .purgem PK4\n .purgem TR8\n .purgem HST\n .purgem HSL\n .purgem PUBLISH\n"
        ".purgem G2RD\n .purgem G2RDS\n .purgem G2AT\n .purgem G2WR\n .purgem W2LD\n .purgem G2LD\n .purgem G2MF\n"
        : "+{v[0:31]}"(acc[0]), "+{v[32:63]}"(acc[1]), "=&s"(s12), "+{v[128:143]}"(va), "+{v[144:159]}"(bb), "+{v[240:255]}"(vb), "+&s"(sh), "=&s"(cnt)
        : "s"(mkd(p.ws, WS_END)), "s"(hs), "s"(tag_of(ep)), "s"(role), "n"(F_STAT), "n"(F_GELU), "n"(F_DONE), "n"(0), "n"(HS),
          "s"(mkd(p.w2, F * D * 2)), "s"(sw), "s"(Lb + w * 4096)
        : "memory", "scc", "vcc", "v64", "v65", "v66", "v67", "v68", "v69", V10(7), V10(8), V10(9), V10(10), V10(11), "v120", "v121", "v122", "v123", "v124", "v125", "v126", "v127",
          V10(16), V10(17), V10(18), V10(19), V10(20), V10(21), V10(22), V10(23)
    );
  }
  if (job) ST(5);
}

constexpr int P_KT = 6 * CONV_BUF, P_KV = P_KT + 24576;
static_assert(P_KV + 3 * KV_IN <= L_FLAG, "lds");
__device__ __forceinline__ float dot2(unsigned a, unsigned b, float c) {
  return __builtin_amdgcn_fdot2_f32_bf16(__builtin_bit_cast(bf2_t, a), __builtin_bit_cast(bf2_t, b), c, false);
}
__device__ __forceinline__ float sum8(float v) {
  v += __int_as_float(__builtin_amdgcn_mov_dpp(__float_as_int(v), 0xb1, 0xf, 0xf, true));
  v += __int_as_float(__builtin_amdgcn_mov_dpp(__float_as_int(v), 0x4e, 0xf, 0xf, true));
  return v + __int_as_float(__builtin_amdgcn_mov_dpp(__float_as_int(v), 0x141, 0xf, 0xf, true));
}
__device__ __forceinline__ void prep_load(const Params& p, LAS char* L, int pp, int w, int lane) {
  const int row = lane >> 3, o = (64 * pp + 32 * (w & 1) + row) * 1280 + 7 * 128 + (((lane & 7) ^ ((row >> 1) & 2)) << 4);
  const int vo[4] = {o ^ 64, o + 9216, (o ^ 64) + 2 * 9216, o + 3 * 9216};
  const v4u rk = mkd(w >= 2 ? p.v : p.k, S * D * 2);
#pragma unroll
  for (int n = 0; n < 3; ++n) {
    dma4(rk, L + P_KV + n * KV_IN + 1024 + w * 4096, vo, n * 128);
    if (w == 0) dma16(rk, L + P_KV + n * KV_IN, row >= 5 ? o - 8 * 1280 : OOB, n * 128);
    if (w == 3) dma16(mkd(p.sk, D * 8), L + P_KV + n * KV_IN + 17408, lane < 32 ? lane * 16 : OOB, (7 + n) * 512);
  }
  __builtin_amdgcn_s_barrier();
}
__device__ __forceinline__ unsigned prep_kv(const Params& p, LAS char* L, int pp, int w, int lane) {
  const int c0 = 448 + 6 * pp;
  u64 ep = ask_epoch(p);
  TS(0);
  KvPre kp;
  kv_pre(kp, L + P_KV, L + P_KT, w, lane);
  TS(6);
  asm volatile("s_waitcnt vmcnt(0) lgkmcnt(0)" ::: "memory");
  lds_post(L, F_GELU + w, 1);
  lds_wait4(L, F_GELU, lane);
  TS(7);
  asm volatile("" : "+s"(ep));
  const u64 tag = tag_of(ep);
  kv_make<3>(kp);
  TS(1);
#pragma unroll
  for (int n = 0; n < 3; ++n) kv_store<2>(p, L + P_KT + n * 8192, 2 * w, pp, 7 + n, lane);
  lds_post(L, F_RING + w, 1);
  TS(2);
  WAIT_V(0);
  set_flag(p, lane < 3 ? flag_at(FK + (28 + 4 * lane + w) * 32 + pp) : OOB, tag);
  TS(3);
  if (w == 3) {
    wait_flag<2>(p, flag_at(FK + ((c0 + (lane < 32 ? 0 : 5)) >> 4) * 32 + (lane & 31)), tag);
    lds_post(L, F_STAT, 1);
  }
  lds_wait<1>(L, F_STAT, 1);
  TS(4);
  stage_u(p, L + w * CONV_BUF, c0 + w, lane);
  if (w < 2) stage_u(p, L + (4 + w) * CONV_BUF, c0 + 4 + w, lane);
  WAIT_V(0);
  lds_post(L, F_STAGED + w, 1);
  TS(5);
  return tag >> 32;
}
__device__ __forceinline__ unsigned prep_side(const Params& p, LAS char* L, int pp, int a, int lane) {
  const int c0 = 448 + 6 * pp + 3 * (a & 1);
  LAS char* Lc = L + 3 * (a & 1) * CONV_BUF;
  u64 ep;
  if (a < 2) {
#pragma unroll
    for (int n = 0; n < 3; ++n) stage_pads(Lc + n * CONV_BUF, lane);
    u64 dd;
    v4u hh[3];
    asm volatile(
        "s_load_dwordx2 %0, %5, 0x0\n s_load_dwordx2 %1, %6, 0x0\n s_load_dwordx4 %2, %7, 0x0\n s_load_dwordx4 %3, %7, 0x1000\n"
        "s_load_dwordx4 %4, %7, 0x2000\n s_waitcnt lgkmcnt(0)"
        : "=&s"(ep), "=&s"(dd), "=&s"(hh[0]), "=&s"(hh[1]), "=&s"(hh[2])
        : "s"(p.ws + WS_EPOCH), "s"(p.dv + (c0 & ~1) * 2), "s"(p.hk + c0 * 4096)
        : "memory");
    dd >>= 16 * a;
#pragma unroll
    for (int c = 0; c < 3; ++c) stage_head(Lc + c * CONV_BUF, hh[c], ((unsigned)(dd >> (16 * c)) & 0xffffu) << 16);
  } else {
    *(LAS int*)(L + L_FLAG + lane * 4) = 0;
    ep = ask_epoch(p);
  }
  BAR();
  asm volatile("" : "+s"(ep));
  const u64 tag = tag_of(ep);
  {
    const rsrc_t rx = mk(p.x, S * D * 2);
    const int row = lane >> 3, c = lane & 7;
    v4u xr[20];
#pragma unroll
    for (int n = 0; n < 20; ++n) xr[n] = __builtin_amdgcn_raw_buffer_load_b128(rx, (64 * pp + 16 * a + row) * 1280 + c * 16 + (n % 10) * 128, (n / 10) * 8 * 1280, 0);
    SB();
    float sx[2], sq[2];
#pragma unroll
    for (int h = 0; h < 2; ++h) {
      sx[h] = sq[h] = 0.f;
#pragma unroll
      for (int n = 0; n < 40; ++n) {
        const unsigned v = xr[10 * h + (n >> 2)][n & 3];
        sx[h] = dot2(v, 0x3f803f80u, sx[h]);
        sq[h] = dot2(v, v, sq[h]);
      }
      sx[h] = sum8(sx[h]);
      sq[h] = sum8(sq[h]);
    }
    const float mean = (c ? sx[1] : sx[0]) * (1.f / D);
    const float var = fmaxf((c ? sq[1] : sq[0]) * (1.f / D) - mean * mean, 0.f);
    const unsigned md = pk(mean, __builtin_amdgcn_sqrtf(rb(rb(var) + 1e-5f)));
    __builtin_amdgcn_raw_buffer_store_b64(v2u{md, (unsigned)(tag >> 32)}, mk(p.ws, WS_END), c < 2 ? WS_STAT + (64 * pp + 16 * a + 8 * c + row) * 8 : OOB, 0, SC1);
    SB();
  }
  TS(0);
  lds_wait4<1>(L, F_RING, lane);
  if (a < 2) {
#pragma unroll
    for (int n = 0; n < 3; ++n) stage_h<0>(p, Lc + n * CONV_BUF, (c0 + n) * 4096 + lane * 16);
    WAIT_V(0);
#pragma unroll
    for (int n = 0; n < 3; ++n) stage_h<1>(p, Lc + n * CONV_BUF, (c0 + n) * 4096 + lane * 16);
    WAIT_V(0);
    lds_post(L, F_STAGED + 4 + a, 1);
  } else {
    touch(p.w2 + (448 * 32 + (2 * pp + a - 2) * 46 + min(lane, 45)) * 128, L + L_DUMP);
  }
  TS(2);
  return tag >> 32;
}

__device__ __forceinline__ void next_epoch(const Params& p) {
  const u64 tag = get_tag(p);
  const int lane = lane_id();
#pragma unroll 1
  for (int spin = 0; spin < SPIN_CAP; ++spin) {
    bool ok = true;
#pragma unroll
    for (int n = 0; n < 4; ++n) {
      const v2u f = __builtin_amdgcn_raw_buffer_load_b64(mk(p.ws, WS_END), flag_at(FK + (lane >> 5) * 128 + (lane & 31)), n * 256 * FSTR, SC1);
      ok &= f.y == (unsigned)(tag >> 32) && f.x == (unsigned)tag;
    }
    if (__all(ok)) break;
    asm volatile("s_sleep 8" ::: "memory");
  }
  set_flag(p, lane == 0 ? WS_EPOCH : OOB, tag);
}

__global__ __launch_bounds__(512, 2) void h3_kernel(Params p) {
  extern __shared__ __attribute__((aligned(16))) char smem[];
  LAS char* L = (LAS char*)smem;
  int lane = threadIdx.x & 63;
  const int w = __builtin_amdgcn_readfirstlane(threadIdx.x >> 6);
  const unsigned bid = blockIdx.x, xcd = bid & 7, r = bid >> 3;
  if (w >= 6) code_prefetch(__builtin_amdgcn_s_getpc() & ~255ull, L + L_DUMP, r + 32 * (w - 6), lane);
  SB();
  const bool g1 = r < 28;
  const int jb = 28 * xcd + r, jx = 28 * (xcd >> 1) + r, rng = (jx * 205) >> 10, g5 = jx - 5 * rng, G2 = 5 * (xcd & 1) + g5, t0 = min(6 * rng, 122);
  const bool job = jx < 110;
  const int rr = (r * 37) >> 8, i = 4 * (xcd >> 1) + rr, j = 7 * (xcd & 1) + (r - 7 * rr);
  if (__builtin_expect(g1 && w < 4, 0)) {
    mma_waves(p, L, i, j, job, t0, G2, w, lane);
    if (bid == 223 && w == 0) next_epoch(p);
    DUMP();
    __builtin_amdgcn_endpgm();
  }
  const int pp = 4 * xcd + (r - 28);
  __builtin_amdgcn_s_setprio(3);
  if (__builtin_expect(g1 && w < 6, 1)) gemm1_load(p, L, i, j, jb, w - 4, lane);
  else if (w < 4) prep_load(p, L, pp, w, lane);
  lane = lane_id();
  ConvPre cp;
  conv_pre(cp, L, L + (g1 ? L_CONV - 2 * CONV_BUF : 0) + (w >> 1) * CONV_BUF, (g1 ? 2 * jb - 2 : 448 + 6 * pp) + (w >> 1), w, lane);
  int so[4], xa = (int)(size_t)L + L_XS + xs_at(w & 3, lane);
  w2_src(so, lane);
  int s5 = g2_step(w & 3, 5) * 40960 + G2 * 128, s13 = g2_step(w & 3, 13) * 40960 + G2 * 128;
  asm volatile("" : "+v"(so[0]), "+v"(so[1]), "+v"(so[2]), "+v"(so[3]), "+v"(xa), "+s"(s5), "+s"(s13));
  unsigned t32;
  v2u xv[NT];
  const v4u rw2 = mkd(p.w2, job ? F * D * 2 : 0);
  if (__builtin_expect(g1, 1)) {
    if (__builtin_expect(w < 6, 1)) t32 = gemm1_load2(p, L, rw2, so, t0, G2, jb, w - 4, lane);
    else t32 = gemm1_side(p, L, i, j, t0, jb, w - 6, lane);
    asm volatile("s_barrier" ::: "memory");
    TS(7);
    dma4(rw2, L + w2_block(5) * 16384 + (w - 4) * 4096, so, s5);
    dma4(rw2, L + w2_block(6) * 16384 + (w - 4) * 4096, so, s5 + 40960);
#pragma unroll
    for (int u = 0; u < NT; ++u) xv[u] = at<v2u>(xa + u * 2048);
  } else {
    if (w < 4) __builtin_amdgcn_s_setprio(2);
    if (w < 4) t32 = prep_kv(p, L, pp, w, lane);
    else t32 = prep_side(p, L, pp, w - 4, lane);
    lds_wait<1>(L, F_STAGED + min(lane & 7, 5), 1);
  }
  u32x32 held;
  asm volatile("" : "={v[224:255]}"(held));
#pragma unroll 1
  for (int round = 0;; ++round) {
    conv(p, cp, w, round, g1, rw2, so[0], s5 + 7 * 40960, s13, held, t32);
    if (__builtin_expect(w >= 4 || round, 1)) break;
    cp.ba += 4 * CONV_BUF;
    cp.aa += 4 * CONV_BUF, cp.lb += 4 * CONV_BUF, cp.cw += 4 * CONV_BUF, cp.kw += 4 * CONV_BUF, cp.yr += 4 * CONV_BUF;
    cp.yo += 4 * YS;
    asm volatile("" : "+v"(cp.ba), "+v"(cp.aa), "+v"(cp.lb), "+v"(cp.cw), "+v"(cp.kw), "+v"(cp.yr), "+v"(cp.yo));
  }
  ST(4);
  if (g1 && job) {
    lane = lane_id();
    lds_wait_at<0, (F_GELU - 4) * 4>(cp.fl, 1);
#pragma unroll
    for (int n = 0; n < 8; ++n)
      *(LAS v4u*)(L + w2_block(12 + n / 4) * 16384 + (w - 4) * 4096 + (n & 3) * 1024 + lane * 16) = v4u{held[4 * n], held[4 * n + 1], held[4 * n + 2], held[4 * n + 3]};
    asm volatile("" ::: "memory");
    at<volatile int>(cp.fl) = 7;
    finish(p, L, xv, t0, rng, G2, w - 4, t32, lane);
  }
  DUMP();
}
}

extern "C" void kernel_launch(void* const* d_in, const int* in_sizes, int n_in, void* d_out, int out_size, void* d_ws, size_t ws_size,
                              hipStream_t stream) {
  using namespace h3;
  Params p = {};
  p.x = (const char*)d_in[0];
  p.k = (const char*)d_in[1];
  p.v = (const char*)d_in[2];
  p.sk = (const char*)d_in[3];
  p.hk = (const char*)d_in[4];
  p.dv = (const char*)d_in[5];
  p.w1 = (const char*)d_in[7];
  p.b1 = (const char*)d_in[8];
  p.w2 = (const char*)d_in[9];
  p.b2 = (const char*)d_in[10];
  p.out = (char*)d_out;
  p.ws = (char*)d_ws;
  (void)hipFuncSetAttribute((const void*)h3_kernel, hipFuncAttributeMaxDynamicSharedMemorySize, LDS_BYTES);
  h3_kernel<<<256, 512, LDS_BYTES, stream>>>(p);
}
```

```cpp
#include <hip/hip_runtime.h>
#include <stdint.h>

namespace h3 {
#define LAS __attribute__((address_space(3)))
#define GAS __attribute__((address_space(1)))
typedef unsigned v4u __attribute__((ext_vector_type(4)));
typedef unsigned v2u __attribute__((ext_vector_type(2)));
typedef float f32x4 __attribute__((ext_vector_type(4)));
typedef float f32x2 __attribute__((ext_vector_type(2)));
typedef float f32x16 __attribute__((ext_vector_type(16)));
typedef float f32x32 __attribute__((ext_vector_type(32)));
typedef unsigned u32x32 __attribute__((ext_vector_type(32)));
typedef int i32x16 __attribute__((ext_vector_type(16)));
typedef int i32x8 __attribute__((ext_vector_type(8)));
typedef short s16x8 __attribute__((ext_vector_type(8)));
typedef short s16x4 __attribute__((ext_vector_type(4)));
typedef __bf16 bf2_t __attribute__((ext_vector_type(2)));
typedef unsigned long long u64;
typedef __amdgpu_buffer_rsrc_t rsrc_t;

constexpr int S = 2048, D = 640, F = 1728, KS2 = 54, NT = 6;
constexpr int SC1 = 16;
#define LAYOUT5
#define LAYOUT11
#define LAYOUT15
constexpr int WS_EPOCH = 0;
constexpr int WS_STAT = 4096;
constexpr int WS_CS = 20480;
constexpr int HS = 129 * 1024;
constexpr int WS_HT = 196608;
constexpr int WS_KVT = WS_HT + KS2 * HS;
constexpr int YS = 22 * 256;
constexpr int WS_YT = WS_KVT + D * S * 2;
constexpr int WS_FL = WS_YT + D * YS;
constexpr int FSTR = 128;
constexpr int FH = 0;
constexpr int FK = 1024;
constexpr int WS_END = WS_FL + 2304 * FSTR;
__device__ __forceinline__ int flag_at(int n) { return WS_FL + n * FSTR; }
constexpr int WS_STAMP = 32 << 20;
constexpr int OOB = 0x40000000;
constexpr int L_W1 = 49152;
constexpr int L_KV = 98304, L_KT = L_KV + 18432;
constexpr int L_XS = 98304;
constexpr int C_A2 = 4128, C_B = 8256, C_BS = 1184, CONV_BUF = C_B + 4 * C_BS;
constexpr int L_CONV = 131072;
constexpr int L_STAT = L_CONV + 2 * CONV_BUF, L_CS = L_STAT + 1024, L_STAT2 = L_CS + 512, L_FLAG = L_STAT2 + 768, L_DUMP = L_FLAG + 256, L_TAG = L_DUMP + 1024, L_B2 = L_TAG + 16;
constexpr int LDS_BYTES = 163840;
static_assert(L_DUMP + 2048 <= LDS_BYTES, "lds");
constexpr int F_PAIR = 0, F_STAT = 12, F_GELU = 16, F_RING = 20, F_STAGED = 24, F_DONE = 32;
#define CODE_BYTES 32512
constexpr int SPIN_CAP = 1 << 15;

struct Params {
  const char *x, *k, *v, *sk, *hk, *dv, *w1, *b1, *w2, *b2;
  char *out, *ws;
};

__device__ __forceinline__ rsrc_t mk(const void* p, int bytes) { return __builtin_amdgcn_make_buffer_rsrc((void*)p, 0, bytes, 0x00020000); }
__device__ __forceinline__ f32x4 mfma(v4u a, v4u b, f32x4 c) {
  return __builtin_amdgcn_mfma_f32_16x16x32_bf16(__builtin_bit_cast(s16x8, a), __builtin_bit_cast(s16x8, b), c, 0, 0, 0);
}
__device__ __forceinline__ v2u trread(LAS char* a) { return __builtin_bit_cast(v2u, __builtin_amdgcn_ds_read_tr16_b64_v4i16((LAS s16x4*)a)); }
__device__ __forceinline__ v4u cat(v2u a, v2u b) { return v4u{a.x, a.y, b.x, b.y}; }
__device__ __forceinline__ float blo(unsigned u) { return __uint_as_float(u << 16); }
__device__ __forceinline__ float bhi(unsigned u) { return __uint_as_float(u & 0xffff0000u); }
__device__ __forceinline__ unsigned pk(float a, float b) {
  bf2_t r;
  r[0] = (__bf16)a;
  r[1] = (__bf16)b;
  return __builtin_bit_cast(unsigned, r);
}
__device__ __forceinline__ float rb(float a) { return blo(pk(a, a)); }
__device__ __forceinline__ unsigned pks(float a, float b) {
  asm("" : "+v"(a));
  asm("" : "+v"(b));
  return pk(a, b);
}
__device__ __forceinline__ v4u mkd(const void* p, int bytes) {
  const u64 a = (u64)p;
  return v4u{(unsigned)a, (unsigned)(a >> 32) & 0xffffu, (unsigned)bytes, 0x00020000u};
}
__device__ __forceinline__ void dma16(v4u rs, LAS char* dst, int voff, int soff) {
  asm volatile("s_mov_b32 m0, %0\n\ts_nop 0\n\tbuffer_load_dwordx4 %1, %2, %3 offen lds" ::"s"((unsigned)(size_t)dst), "v"(voff), "s"(rs), "s"(soff)
               : "memory");
}
__device__ __forceinline__ void dma16c(v4u rs, LAS char* dst, int voff, int soff) {
  asm volatile("s_mov_b32 m0, %0\n\ts_nop 0\n\tbuffer_load_dwordx4 %1, %2, %3 offen sc1 lds" ::"s"((unsigned)(size_t)dst), "v"(voff), "s"(rs), "s"(soff)
               : "memory");
}
__device__ __forceinline__ void dma4(v4u rs, LAS char* dst, const int (&vo)[4], int soff) {
  asm volatile(
      "s_mov_b32 m0, %0\n\ts_nop 0\n\tbuffer_load_dwordx4 %1, %5, %6 offen lds\n\tbuffer_load_dwordx4 %2, %5, %6 offen offset:1024 lds\n\t"
      "buffer_load_dwordx4 %3, %5, %6 offen offset:2048 lds\n\tbuffer_load_dwordx4 %4, %5, %6 offen offset:3072 lds" ::"s"((unsigned)(size_t)dst),
      "v"(vo[0]), "v"(vo[1]), "v"(vo[2]), "v"(vo[3]), "s"(rs), "s"(soff)
      : "memory");
}
template <int AUX>
__device__ __forceinline__ void st128(v4u v, rsrc_t rs, int voff) { __builtin_amdgcn_raw_buffer_store_b128(v, rs, voff, 0, AUX); }
#define WAIT_V(n) asm volatile("s_waitcnt vmcnt(" #n ")" ::: "memory")
#define WAIT_L0() asm volatile("s_waitcnt lgkmcnt(0)" ::: "memory")
#define BAR()                          \
  do {                                 \
    WAIT_L0();                         \
    __builtin_amdgcn_s_barrier();      \
    asm volatile("" ::: "memory");     \
  } while (0)
#define SB() __builtin_amdgcn_sched_barrier(0)
#define CLK "s_memrealtime"
#define RAW(idx, v)
#define ST(idx)
#define TS(k)
#define DUMP()

__device__ __forceinline__ int lane_id() {
  int l;
  asm volatile("v_mbcnt_lo_u32_b32 %0, -1, 0\n\tv_mbcnt_hi_u32_b32 %0, -1, %0" : "=v"(l));
  return l;
}
template <int NAP = 0>
__device__ __forceinline__ void wait_flag(const Params& p, int off, u64 tag) {
  int cnt = SPIN_CAP;
  u64 f;
  asm volatile(
      "1:\n"
      "buffer_load_dwordx2 %0, %2, %3, 0 offen sc1\n"
      "s_sub_u32 %1, %1, 1\n"
      "s_cbranch_scc1 2f\n"
      "s_sleep %5\n"
      "s_waitcnt vmcnt(0)\n"
      "v_cmp_ne_u64 vcc, %4, %0\n"
      "s_mov_b64 exec, vcc\n"
      "s_cbranch_vccnz 1b\n"
      "2:\n"
      "s_mov_b64 exec, -1\n"
      "s_waitcnt vmcnt(0)\n"
      : "=&v"(f), "+s"(cnt)
      : "v"(off), "s"(mkd(p.ws, WS_END)), "s"(tag), "n"(NAP)
      : "memory", "vcc", "scc");
}
__device__ __forceinline__ void set_flag(const Params& p, int off, u64 tag) {
  __builtin_amdgcn_raw_buffer_store_b64(v2u{(unsigned)tag, (unsigned)(tag >> 32)}, mk(p.ws, WS_END), off, 0, SC1);
}
__device__ __forceinline__ void lds_post(LAS char* L, int idx, int val) {
  asm volatile("" ::: "memory");
  *(volatile LAS int*)(L + L_FLAG + idx * 4) = val;
}
__device__ __forceinline__ void lds_post2(LAS char* L, int idx) {
  asm volatile("" ::: "memory");
  *(volatile LAS v2u*)(L + L_FLAG + idx * 4) = v2u{1u, 1u};
}
template <int NAP = 0, int OFF = 0>
__device__ __forceinline__ void lds_wait_at(int a, int val) {
  int cnt = 1 << 22, f;
  asm volatile(
      "1:\n"
      "ds_read_b32 %0, %2 offset:%5\n"
      "s_sub_u32 %1, %1, 1\n"
      "s_cbranch_scc1 2f\n"
      ".if %4\n s_sleep %4\n .endif\n"
      "s_waitcnt lgkmcnt(0)\n"
      "v_cmp_gt_i32 vcc, %3, %0\n"
      "s_cbranch_vccnz 1b\n"
      "2:\n"
      "s_waitcnt lgkmcnt(0)\n"
      : "=&v"(f), "+s"(cnt)
      : "v"(a), "s"(val), "n"(NAP), "n"(OFF)
      : "memory", "vcc", "scc");
}
template <int NAP = 0>
__device__ __forceinline__ void lds_wait(LAS char* L, int idx, int val) { lds_wait_at<NAP>((int)(size_t)(L + L_FLAG + idx * 4), val); }
template <int NAP = 0>
__device__ __forceinline__ void lds_wait4(LAS char* L, int idx, int lane) { lds_wait<NAP>(L, idx + (lane & 3), 1); }
__device__ __forceinline__ u64 tag_of(u64 e) { return e * 6364136223846793005ull + 1442695040888963407ull; }
__device__ __forceinline__ u64 ask_epoch(const Params& p) {
  u64 e;
  asm volatile("s_load_dwordx2 %0, %1, 0x0" : "=s"(e) : "s"(p.ws + WS_EPOCH) : "memory");
  return e;
}
__device__ __forceinline__ u64 get_tag(const Params& p) {
  u64 e;
  asm volatile("s_load_dwordx2 %0, %1, 0x0\n\ts_waitcnt lgkmcnt(0)" : "=s"(e) : "s"(p.ws + WS_EPOCH) : "memory");
  return tag_of(e);
}
__device__ __forceinline__ void touch(const char* a, LAS char* dump) {
  asm volatile("s_mov_b32 m0, %0\n\ts_nop 0\n\tglobal_load_lds_dword %1, off" ::"s"((unsigned)(size_t)dump), "v"(a) : "memory");
}
__device__ __forceinline__ void code_prefetch(u64 pc, LAS char* dump, int r, int lane) {
  dma16(mkd((const void*)pc, CODE_BYTES), dump, r * 1024 + lane * 16, 0);
}

constexpr int KV_IN = 18432;
struct KvPre {
  v4u m, sf;
  int sel;
  LAS char *ka[2], *va, *sa, *ta[2];
};
__device__ __forceinline__ void kv_pre(KvPre& k, LAS char* Li, LAS char* Lt, int cb, int lane) {
  const int c = lane & 15, g = lane >> 4, tok = 4 * (c & 3) + (c >> 2);
#pragma unroll
  for (int d = 0; d < 4; ++d) k.m[d] = (g & 1) == (c >> 3) && d == ((c >> 1) & 3) ? (c & 1 ? 0xffff0000u : 0xffffu) : 0u;
  k.sf = k.m & ((g >> 1) == (cb & 1) ? 0x3f803f80u : 0u);
  k.sel = g & 2 ? 0x03020302 : 0x01000100;
#pragma unroll
  for (int y = 0; y < 2; ++y) {
    const int row = 8 + tok - 2 * y - (g >> 1);
    k.ka[y] = Li + row * 128 + (((2 * cb + (g & 1)) ^ ((row >> 1) & 6)) << 4);
  }
  k.va = Li + 9216 + tok * 128 + (((4 * (cb >> 1) + g) ^ 4 ^ ((tok >> 1) & 6)) << 4);
  k.sa = Li + 17408 + cb * 128 + c * 8;
  k.ta[0] = Lt + (16 * cb + c) * 128 + (((2 * g) ^ (c & 7)) << 4);
  k.ta[1] = Lt + (16 * cb + c) * 128 + (((2 * g + 1) ^ (c & 7)) << 4);
  asm volatile("" : "+v"(k.m), "+v"(k.sf), "+v"(k.sel), "+v"(k.ka[0]), "+v"(k.ka[1]), "+v"(k.va), "+v"(k.sa), "+v"(k.ta[0]), "+v"(k.ta[1]));
}
template <int N>
__device__ __forceinline__ void kv_make(const KvPre& k) {
  v2u sk[N];
  v4u kf[N][4][2], vf[N][4];
#pragma unroll
  for (int n = 0; n < N; ++n) {
    sk[n] = *(LAS v2u*)(k.sa + n * KV_IN);
#pragma unroll
    for (int q = 0; q < 4; ++q) {
      vf[n][q] = *(LAS v4u*)(k.va + n * KV_IN + q * 2048);
#pragma unroll
      for (int y = 0; y < 2; ++y) kf[n][q][y] = *(LAS v4u*)(k.ka[y] + n * KV_IN + q * 2048);
    }
  }
  SB();
#pragma unroll
  for (int n = 0; n < N; ++n) {
    const v4u w0 = k.m & __builtin_amdgcn_perm(sk[n].x, sk[n].x, k.sel), w1 = k.m & __builtin_amdgcn_perm(sk[n].y, sk[n].y, k.sel);
    const f32x4 Z = {0.f, 0.f, 0.f, 0.f};
    v2u o2[4];
#pragma unroll
    for (int q = 0; q < 4; ++q) {
      const f32x4 o = mfma(kf[n][q][1], w1, mfma(kf[n][q][0], w0, Z)) * mfma(vf[n][q], k.sf, Z);
      o2[q] = v2u{pk(o[0], o[1]), pk(o[2], o[3])};
    }
    *(LAS v4u*)(k.ta[0] + n * 8192) = cat(o2[0], o2[1]);
    *(LAS v4u*)(k.ta[1] + n * 8192) = cat(o2[2], o2[3]);
  }
}
template <int N>
__device__ __forceinline__ void kv_store(const Params& p, LAS char* Lt, int n0, int pp, int G, int lane) {
  const int row = lane >> 3;
#pragma unroll
  for (int n = 0; n < N; ++n)
    st128<SC1>(*(LAS v4u*)(Lt + (n0 + n) * 1024 + lane * 16), mk(p.ws, WS_END),
               WS_KVT + (64 * G + 8 * (n0 + n) + row) * 4096 + 128 * pp + (((lane & 7) ^ row) << 4));
}

__device__ __forceinline__ int g2_step(int w, int rot, int k) {
  return k == 13 ? 26 + 27 * (w >> 1) : 27 * (w >> 1) + 13 * (w & 1) + (k + rot >= 13 ? k + rot - 13 : k + rot);
}
__device__ __forceinline__ int w2_block(int k) { return (0x63052417u >> (4 * (k & 7))) & 7; }
__device__ __forceinline__ void w2_src(int (&so)[4], int lane) {
  const int row = lane >> 3;
#pragma unroll
  for (int h = 0; h < 4; ++h) so[h] = (8 * h + row) * 1280 - 1024 * h + (((lane & 7) ^ (((row >> 1) & 3) << 1)) << 4);
}
__device__ __forceinline__ void w2_fetch(v4u rw2, LAS char* L, const int (&so)[4], int w, int rot, int k, int G2) {
  dma4(rw2, L + w2_block(k) * 16384 + w * 4096, so, g2_step(w, rot, k) * (32 * 1280) + G2 * 128);
}

__device__ __forceinline__ void stage_pads(LAS char* Lc, int lane) {
  if (lane < 16) *(LAS unsigned*)(Lc + (lane >> 3) * C_A2 + (lane & 7) * 4) = 0u;
  *(LAS v2u*)(Lc + C_B + (lane >> 4) * C_BS + (lane & 15) * 8) = v2u{0u, 0u};
}
template <int DELAYED>
__device__ __forceinline__ void stage_h(const Params& p, LAS char* Lc, int o) {
  asm volatile(
      "s_mov_b32 m0, %0\n\ts_mov_b64 exec, -2\n\tbuffer_load_dwordx4 %1, %2, 0 offen lds\n\ts_mov_b64 exec, -1\n\t"
      "buffer_load_dwordx4 %1, %2, 0 offen offset:1024 lds\n\tbuffer_load_dwordx4 %1, %2, 0 offen offset:2048 lds\n\t"
      "buffer_load_dwordx4 %1, %2, 0 offen offset:3072 lds" ::"s"((unsigned)(size_t)(Lc + DELAYED * C_A2 + 32)),
      "v"(o), "s"(mkd(p.hk - 4 * DELAYED, D * S * 2 + 4 * DELAYED))
      : "memory");
}
__device__ __forceinline__ int u_src(int ch, int lane) { return WS_KVT + ch * 4096 + (lane >> 3) * 512 + (lane & 7) * 16; }
__device__ __forceinline__ void stage_u(const Params& p, LAS char* Lc, int o) {
  asm volatile(
      "s_mov_b32 m0, %0\n s_nop 0\n"
      ".set e, 0\n .rept 4\n buffer_load_dwordx4 %1, %2, 0 offen offset:e*128 sc1 lds\n"
      " .if e < 3\n s_add_u32 m0, m0, %3\n s_nop 0\n .endif\n .set e, e+1\n .endr\n" ::"s"((unsigned)(size_t)(Lc + C_B + 128)),
      "v"(o), "s"(mkd(p.ws, WS_END)), "n"(C_BS - 128)
      : "memory", "scc");
}
__device__ __forceinline__ void stage_head(LAS char* Lc, v4u h, unsigned dbits) {
  const unsigned h01 = pk(blo(h.x) + __uint_as_float(dbits), bhi(h.x));
  *(LAS v4u*)(Lc + 32) = v4u{h01, h.y, h.z, h.w};
  *(LAS v4u*)(Lc + C_A2 + 32) = v4u{0u, h01, h.y, h.z};
}
#define OUT_AUX 0
#define FK_AT 2
#define H_PRIO 0
#define L_NB 3
#define ROT 0
#define E_YST 3
#define TCH 1
#define RI 1
#define RJ 1
__device__ __forceinline__ int rot_of(int i, int j) { return (RI * i + RJ * j) % 10; }
__device__ __forceinline__ void kv_load(const Params& p, LAS char* L, int jb, int lw, int lane) {
  const int row = lane >> 3, o = (64 * (jb & 31) + row) * 1280 + (jb >> 5) * 128 + (((lane & 7) ^ ((row >> 1) & 2)) << 4);
  const int vo[4] = {o ^ 64, o + 9216, (o ^ 64) + 2 * 9216, o + 3 * 9216};
  const v4u rk = mkd(lw ? p.v : p.k, S * D * 2);
  LAS char* d = L + L_KV + 1024 + lw * 8192;
  dma4(rk, d, vo, 0);
  dma4(rk, d + 4096, vo, 32 * 1280);
  if (lw) dma16(mkd(p.sk, D * 8), L + L_KV + 17408, lane < 32 ? (jb >> 5) * 512 + lane * 16 : OOB, 0);
  else dma16(rk, L + L_KV, row >= 5 ? o - 8 * 1280 : OOB, 0);
}
struct LoadPre {
  int xo[4], wo[4], kst;
};
__device__ __forceinline__ void gemm1_first(const Params& p, LAS char* L, LoadPre& q, int i, int j, int jb, int lw, int lane) {
  kv_load(p, L, jb, lw, lane);
  const int i16 = lane & 15, g = lane >> 4;
#pragma unroll
  for (int e = 0; e < 4; ++e) {
    q.xo[e] = (128 * i + 64 * lw + 8 * e + (lane >> 3)) * 1280 - 1024 * e + (((lane & 7) ^ (4 * (e & 1) + g)) << 4);
    const int c = i16 ^ ((g | ((e >> 1) << 2)) << 1);
    q.wo[e] = (32 * lw + 4 * e + g) * 3456 - 1024 * e + 256 * j + (c << 4);
    if (j == 13 && c >= 8) q.wo[e] = OOB;
  }
  q.kst = rot_of(i, j);
  TS(0);
#pragma unroll
  for (int h = 0; h < 2; ++h) {
    dma4(mkd(p.x, S * D * 2), L + lw * 8192 + h * 4096, q.xo, q.kst * 128 + h * 32 * 1280);
    dma4(mkd(p.w1, D * F * 2), L + L_W1 + lw * 8192 + h * 4096, q.wo, q.kst * (64 * 3456) + h * 16 * 3456);
  }
}
__device__ __forceinline__ unsigned gemm1_load(const Params& p, LAS char* L, const LoadPre& q, v4u rw2, const int (&so)[4], int t0, int G2, int rot, int jb, int lw,
                                               int lane) {
  int ho = (2 * jb + lw) * 4096 + lane * 16;
  int xs = (16 * t0 + 48 * lw + (lane >> 3)) * 1280 + G2 * 128 + (((lane & 7) ^ (lane >> 3)) << 4);
  asm volatile("" : "+v"(ho), "+v"(xs));
  const v4u rx = mkd(p.x, S * D * 2), rw = mkd(p.w1, D * F * 2);
  {
    const int k1 = q.kst == 9 ? 0 : q.kst + 1, mb = (int)(size_t)L + lw * 8192;
    int sx = k1 * 128, sw = k1 * (64 * 3456), sm = mb + 16384, cnt = 9, t0, t1, t2;
#define LD4(a, b, c, d, r, s)                                                                                            \
  "buffer_load_dwordx4 %" #a ", %" #r ", %" #s " offen lds\n buffer_load_dwordx4 %" #b ", %" #r ", %" #s " offen offset:1024 lds\n" \
  "buffer_load_dwordx4 %" #c ", %" #r ", %" #s " offen offset:2048 lds\n buffer_load_dwordx4 %" #d ", %" #r ", %" #s " offen offset:3072 lds\n"
    asm volatile(
        "s_branch 2f\n"
        "1:\n" LD4(8, 9, 10, 11, 16, 4)
        "s_add_u32 m0, %6, 49152\n"
        "s_nop 0\n" LD4(12, 13, 14, 15, 17, 5)
        "2:\n"
        "s_mov_b32 m0, %2\n"
        "s_sub_u32 %3, %3, 1\n"
        "s_waitcnt vmcnt(16)\n"
        "s_barrier\n"
        "s_cbranch_scc1 3f\n" LD4(8, 9, 10, 11, 16, 0)
        "s_add_u32 m0, %2, 49152\n"
        "s_add_u32 %4, %0, 32*1280\n" LD4(12, 13, 14, 15, 17, 1)
        "s_add_u32 %6, %2, 4096\n"
        "s_mov_b32 m0, %6\n"
        "s_add_u32 %5, %1, 16*3456\n"
        "s_add_u32 %0, %0, 128\n"
        "s_add_u32 %1, %1, 64*3456\n"
        "s_cmp_eq_u32 %0, 1280\n"
        "s_cselect_b32 %0, 0, %0\n"
        "s_cselect_b32 %1, 0, %1\n"
        "s_add_u32 %2, %2, 16384\n"
        "s_cmp_eq_u32 %2, %7\n"
        "s_cselect_b32 %2, %18, %2\n"
        "s_branch 1b\n"
        "3:\n"
        : "+&s"(sx), "+&s"(sw), "+&s"(sm), "+&s"(cnt), "=&s"(t0), "=&s"(t1), "=&s"(t2)
        : "s"(mb + 49152), "v"(q.xo[0]), "v"(q.xo[1]), "v"(q.xo[2]), "v"(q.xo[3]), "v"(q.wo[0]), "v"(q.wo[1]), "v"(q.wo[2]), "v"(q.wo[3]), "s"(rx),
          "s"(rw), "s"(mb)
        : "memory", "scc"
        );
  }
  TS(4);
  v2u tg = *(volatile LAS v2u*)(L + L_TAG);
  asm volatile("s_waitcnt vmcnt(0)\n s_barrier" : "+v"(tg)::"memory");
  stage_h<1>(p, L + L_CONV + lw * CONV_BUF, ho);
#pragma unroll
  for (int h = 0; h < 6; ++h) dma16(rx, L + L_XS + (6 * lw + h) * 1024, xs, h * 8 * 1280);
  TS(5);
#pragma unroll
  for (int k = 0; k < 5; ++k) {
    if (k == L_NB) {
      TS(2);
      asm volatile("s_waitcnt vmcnt(%0)\n s_barrier" ::"n"(8 * L_NB) : "memory");
    }
    w2_fetch(rw2, L, so, 2 * lw, rot, k, G2);
    w2_fetch(rw2, L, so, 2 * lw + 1, rot, k, G2);
  }
  TS(1);
  return __builtin_amdgcn_readfirstlane(tg.y);
}

__device__ __forceinline__ unsigned gemm1_side(const Params& p, LAS char* L, int i, int j, int t0, int jb, int sw, int lane) {
  LAS char* Lc = L + L_CONV + sw * CONV_BUF;
  const int ch = 2 * jb + sw;
  if (sw) *(LAS int*)(L + L_FLAG + lane * 4) = 0;
  stage_pads(Lc, lane);
  u64 ep;
  unsigned db;
  v4u hh;
  asm volatile("s_load_dwordx2 %0, %3, 0x0\n s_load_dword %1, %4, 0x0\n s_load_dwordx4 %2, %5, 0x0"
               : "=&s"(ep), "=&s"(db), "=&s"(hh)
               : "s"(p.ws + WS_EPOCH), "s"(p.dv + jb * 4), "s"(p.hk + ch * 4096)
               : "memory");
  const int g = lane >> 4, q = (lane >> 2) & 3;
  const char* tch = p.w2 + jb * 8192 + lane * 128;
  int o0 = WS_STAT + (128 * i + 64 * sw + lane) * 8, o1 = WS_CS + (128 * j + 64 * sw + lane) * 8;
  int o2 = WS_STAT + (16 * t0 + 48 * sw + min(lane, 47)) * 8, o3 = sw ? OOB : flag_at(FK + (ch >> 4) * 32 + (lane & 31));
  int ho = sw ? jb * 8192 + lane * 16 : u_src(ch, lane), ho2 = ho + 4096;
  LAS char *d0 = L + L_STAT + (64 * sw + lane) * 8, *d1 = L + L_CS + (64 * sw + lane) * 4;
  LAS char* d2 = L + L_STAT2 + (48 * sw + min(lane, 47)) * 8;
  int of = lane < 2 ? flag_at(FK + (4 * (jb >> 5) + 2 * sw + lane) * 32 + (jb & 31)) : OOB;
  int oc = i < 10 && !(i & 1) ? o1 + (i >> 1) * 14336 : OOB;
  int ko[4], ca[4], ka = (int)(size_t)L + L_KT + sw * 4096 + lane * 16;
  v4u one[4];
#pragma unroll
  for (int u = 0; u < 4; ++u) {
    ko[u] = WS_KVT + (64 * (jb >> 5) + 32 * sw + 8 * u + (lane >> 3)) * 4096 + 128 * (jb & 31) + (((lane & 7) ^ (lane >> 3)) << 4);
    ca[u] = (int)(size_t)L + L_W1 + (8 * g + q) * 256 + (((4 * sw + u) ^ (q | ((g & 1) << 2))) << 5) + 8 * (lane & 3);
    const unsigned o = q == u ? 0x3f803f80u : 0u;
    one[u] = v4u{o, o, o, o};
    asm volatile("" : "+v"(ko[u]), "+v"(ca[u]), "+v"(one[u]));
  }
  asm volatile("" : "+v"(tch), "+v"(o0), "+v"(o1), "+v"(o2), "+v"(o3), "+v"(ho), "+v"(ho2), "+v"(d0), "+v"(d1), "+v"(d2), "+v"(of), "+v"(oc), "+v"(ka));
  asm volatile("s_barrier\n s_waitcnt lgkmcnt(0)" : "+s"(ep), "+s"(db), "+s"(hh)::"memory");
  stage_head(Lc, hh, sw ? db & 0xffff0000u : db << 16);
  const u64 tag = tag_of(ep);
  const unsigned t32 = (unsigned)(tag >> 32);
  *(LAS v2u*)(L + L_TAG) = v2u{(unsigned)tag, t32};
  const v4u rwd = mkd(p.ws, WS_END);
  BAR();
  {
    v4u sa[2][4][2], kt[4];
    auto rd = [&](int s) {
#pragma unroll
      for (int u = 0; u < 4; ++u)
#pragma unroll
        for (int ks = 0; ks < 2; ++ks) {
          LAS char* aa = (LAS char*)(size_t)ca[u] + s * 16384 + ks * 8192;
          sa[s][u][ks] = cat(trread(aa), trread(aa + 1024));
        }
      SB();
    };
    f32x4 col = {0.f, 0.f, 0.f, 0.f};
    auto mm = [&](int s) {
#pragma unroll
      for (int u = 0; u < 4; ++u)
#pragma unroll
        for (int ks = 0; ks < 2; ++ks) col = mfma(one[u], sa[s][u][ks], col);
      SB();
    };
#pragma unroll
    for (int u = 0; u < 4; ++u) kt[u] = *(LAS v4u*)(size_t)(ka + u * 1024);
    rd(0);
    BAR();
    SB();
    rd(1);
#pragma unroll
    for (int u = 0; u < 4; ++u) st128<SC1>(kt[u], mk(p.ws, WS_END), ko[u]);
    SB();
    BAR();
    SB();
    mm(0);
    mm(1);
    asm volatile("s_barrier\n"
                 "s_waitcnt vmcnt(0)" : "+v"(col)::"memory");
    set_flag(p, of, tag);
    __builtin_amdgcn_raw_buffer_store_b64(v2u{__float_as_uint(col[0]), t32}, mk(p.ws, WS_END), oc, 0, SC1);
    SB();
  }
  TS(2);
  v2u g0, g2, g1[5];
  u64 fk;
#define S_FETCH                                                                                                        \
  "buffer_load_dwordx2 %0, %10, %18, 0 offen sc1\n buffer_load_dwordx2 %1, %11, %18, 0 offen sc1\n"                    \
  "buffer_load_dwordx2 %2, %12, %18, 0 offen sc1\n buffer_load_dwordx2 %3, %13, %18, 0 offen sc1\n"                    \
  "buffer_load_dwordx2 %4, %14, %18, 0 offen sc1\n buffer_load_dwordx2 %5, %15, %18, 0 offen sc1\n"                    \
  "buffer_load_dwordx2 %6, %16, %18, 0 offen sc1\n buffer_load_dwordx2 %7, %17, %18, 0 offen sc1\n"
#define S_OUT "=&v"(g0), "=&v"(g1[0]), "=&v"(g1[1]), "=&v"(g1[2]), "=&v"(g1[3]), "=&v"(g1[4]), "=&v"(g2), "=&v"(fk)
#define S_IN "v"(o0), "v"(o1), "v"(o1 + 14336), "v"(o1 + 2 * 14336), "v"(o1 + 3 * 14336), "v"(o1 + 4 * 14336), "v"(o2), "v"(o3), "s"(rwd)
  asm volatile(
      "s_barrier\n"
      "s_barrier\n s_barrier\n s_barrier\n" S_FETCH
      "s_cmp_eq_u32 %8, 0\n"
      "s_mov_b32 m0, %9\n"
      "s_barrier\n"
      "s_cbranch_scc1 1f\n"
      ".set c, 0\n .rept 2\n"
      " s_mov_b64 exec, -2\n buffer_load_dwordx4 %19, %21, 0 offen lds\n s_mov_b64 exec, -1\n"
      " buffer_load_dwordx4 %19, %21, 0 offen offset:1024 lds\n buffer_load_dwordx4 %19, %21, 0 offen offset:2048 lds\n"
      " buffer_load_dwordx4 %19, %21, 0 offen offset:3072 lds\n"
      " s_add_u32 m0, m0, %24+c*(%25-%24)\n v_mov_b32 %19, %20\n"
      " .set c, c+1\n"
      ".endr\n"
      "global_load_lds_dword %22, off\n"
      "s_waitcnt vmcnt(9)\n"
      "v_mov_b64 %7, %23\n"
      "s_branch 2f\n"
      "1:\n"
      "s_waitcnt vmcnt(0)\n"
      "v_cmp_ne_u64 vcc, %23, %7\n"
      "s_cbranch_vccnz 2f\n"
      ".set e, 0\n .rept 8\n"
      " .if e < 4\n buffer_load_dwordx4 %19, %18, 0 offen offset:e*128 sc1 lds\n .else\n buffer_load_dwordx4 %20, %18, 0 offen offset:(e-4)*128 sc1 lds\n .endif\n"
      " .if e == 3\n s_add_u32 m0, m0, %24-3*%26\n s_nop 0\n .elseif e < 7\n s_add_u32 m0, m0, %26\n s_nop 0\n .endif\n"
      " .set e, e+1\n"
      ".endr\n"
      "2:\n"
      : S_OUT
      : "s"(sw), "s"((unsigned)(size_t)(L + L_CONV + (sw ? 32 : C_B + 128))), S_IN, "v"(ho), "v"(ho2), "s"(mkd(p.hk, D * S * 2)), "v"(tch), "s"(tag),
        "n"(CONV_BUF), "n"(L_DUMP - L_CONV - CONV_BUF - 32), "n"(C_BS - 128)
      : "memory", "scc", "vcc");
  const bool kv_there = __builtin_amdgcn_ballot_w64(fk != tag) == 0;
  TS(3);
  auto stale = [&]() {
    unsigned a = g0.y & g2.y, o = g0.y | g2.y;
#pragma unroll
    for (int n = 0; n < 5; ++n) {
      a &= g1[n].y;
      o |= g1[n].y;
    }
    return __builtin_amdgcn_ballot_w64(a != t32 || o != t32) != 0;
  };
  f32x2 st0, st2;
  float cs;
  auto digest = [&]() {
    cs = __uint_as_float(g1[0].x);
#pragma unroll
    for (int n = 1; n < 5; ++n) cs += __uint_as_float(g1[n].x);
    st0 = f32x2{blo(g0.x), __builtin_amdgcn_rcpf(bhi(g0.x))};
    st2 = f32x2{blo(g2.x), __builtin_amdgcn_rcpf(bhi(g2.x))};
  };
  const bool late = stale();
  digest();
  asm volatile("s_barrier" : "+v"(st0), "+v"(st2), "+v"(cs)::"memory");
  if (__builtin_expect(late, 0)) {
    int spin = SPIN_CAP;
#pragma unroll 1
    do asm volatile(S_FETCH "s_waitcnt vmcnt(0)" : S_OUT : "n"(0), "n"(0), S_IN : "memory");
    while (stale() && --spin);
    digest();
  }
  *(LAS f32x2*)d0 = st0;
  *(LAS float*)d1 = cs;
  lds_post2(L, F_STAT + 2 * sw);
  *(LAS f32x2*)d2 = st2;
  TS(5);
  RAW(14, __builtin_amdgcn_readfirstlane(kv_there + 1000 * late));
  if (__builtin_expect(!kv_there, 0)) {
    wait_flag<2>(p, o3, tag);
    stage_u(p, L + L_CONV, ho);
    stage_u(p, L + L_CONV + CONV_BUF, ho2);
  }
  asm volatile("s_waitcnt vmcnt(0) lgkmcnt(0)" ::: "memory");
  TS(6);
  asm volatile("s_barrier" ::: "memory");
  TS(4);
  return t32;
}

struct ConvPre {
  v4u ba;
  int aa, fl, lb, cw, kw, yr, yo;
};
__device__ __forceinline__ void conv_pre(ConvPre& c, LAS char* L, LAS char* Lc, int ch, int me, int lane) {
  const int pw = me & 1, col = lane & 15, g = lane >> 4, b = (int)(size_t)Lc;
  c.aa = b + pw * C_A2 + 32 + 64 * g - 8 * ((lane >> 2) & 3) + 8 * (lane & 3);
#pragma unroll
  for (int e = 0; e < 4; ++e) {
    const int u = 7 + col - 2 * g + 8 * (e >> 1) + (e & 1);
    c.ba[e] = b + C_B + pw * 64 + ((u >> 2) & 3) * C_BS + (u >> 4) * 128 + (u & 3) * 8;
  }
  c.fl = (int)(size_t)L + L_FLAG + (F_PAIR + me) * 4;
  c.lb = b + lane * 16;
  const int Q = (4 * col + g + 1) & 63;
  c.cw = b + C_B + pw * 2304 + (Q & 3) * 256 + (Q >> 2) * 16;
  c.kw = b + 2048 + pw * 2048 + 32 * col + 8 * g;
  c.yr = b + 2048 + (11 * pw + g) * 192 + 8 * col;
  c.yo = WS_YT + ch * YS + pw * 11 * 256 + lane * 16;
  asm volatile("" : "+v"(c.ba), "+v"(c.aa), "+v"(c.fl), "+v"(c.lb), "+v"(c.cw), "+v"(c.kw), "+v"(c.yr), "+v"(c.yo));
}
template <typename T>
__device__ __forceinline__ LAS T& at(int a) { return *(LAS T*)(size_t)a; }
template <int H>
__device__ __forceinline__ void conv_send(const ConvPre& cp, const f32x32& z0) {
#pragma unroll
  for (int c = 4 * H; c < 4 * H + 4; ++c) at<f32x4>(cp.lb + c * 1024) = f32x4{z0[4 * c], z0[4 * c + 1], z0[4 * c + 2], z0[4 * c + 3]};
  asm volatile("; half %0" ::"n"(H) : "memory");
}
template <int H>
__device__ __forceinline__ void conv_keep(const ConvPre& cp, const f32x32& z0) {
  f32x4 y[4];
  const f32x4 c0 = at<f32x4>(cp.lb + C_B + H * 1024), c1 = at<f32x4>(cp.lb + C_B + 2304 + H * 1024);
#pragma unroll
  for (int c = 0; c < 4; ++c) y[c] = at<f32x4>(cp.lb + (4 * H + c) * 1024);
  SB();
#pragma unroll
  for (int c = 0; c < 4; ++c) {
    const int b = 16 * H + 4 * c;
    at<v2u>(cp.kw + 512 * c) = v2u{pks(y[c][0] + z0[b] + c0[c] + c1[c], y[c][1] + z0[b + 1]), pks(y[c][2] + z0[b + 2], y[c][3] + z0[b + 3])};
  }
  asm volatile("; half %0" ::"n"(H) : "memory");
}
__device__ __forceinline__ void pair_sync(const ConvPre& cp, int val) {
  asm volatile("" ::: "memory");
  at<volatile int>(cp.fl) = val;
  lds_wait_at(cp.fl ^ 4, val);
}
__device__ __forceinline__ void conv(const Params& p, const ConvPre& cp, int me, int round, bool g1, u32x32& held, unsigned t32) {
  const int pw = me & 1;
  u32x32 a0, a1, b0, b1;
  asm volatile(
      ".set n, 0\n"
      ".rept 16\n"
      " ds_read_b64_tr_b16 v[4*n:4*n+1], %4 offset:256*n+32\n"
      " ds_read_b64_tr_b16 v[4*n+2:4*n+3], %4 offset:256*n\n"
      " ds_read_b64 v[64+4*n:65+4*n], v[192+2*(n&1)] offset:32+128*(n/2)\n"
      " ds_read_b64 v[66+4*n:67+4*n], v[193+2*(n&1)] offset:32+128*(n/2)\n"
      " .set n, n+1\n"
      ".endr\n"
      : "=&{v[0:31]}"(a0), "=&{v[32:63]}"(a1), "=&{v[64:95]}"(b0), "=&{v[96:127]}"(b1)
      : "v"(cp.aa), "{v[192:195]}"(cp.ba)
      : "memory");
  if (__builtin_expect(g1, 1)) {
    asm volatile("s_waitcnt vmcnt(8) lgkmcnt(0)" ::: "memory");
    ST(8);
    asm volatile("s_barrier" ::: "memory");
  } else if (me >= 4) {
    int a = cp.fl;
    asm volatile("" : "+v"(a));
    lds_wait_at<2>(a - 16, 2);
  }
  ST(9);
  f32x32 z1, z0;
  asm volatile(
      "s_waitcnt lgkmcnt(0)\n"
      "s_setprio 1\n"
      ".set m, 0\n"
      ".set r, 0\n"
      ".set y, 0\n"
      ".rept 2\n"
      " .set c, 7\n"
      " .rept 8\n"
      "  .set n, 0\n"
      "  .rept 2*c+2\n"
      "   .if m == 72\n"
      "    s_waitcnt lgkmcnt(4)\n"
      "   .endif\n"
      "   .if m == 84\n"
      "    s_waitcnt lgkmcnt(0)\n"
      "    ds_write_b32 %8, %9\n"
      "   .endif\n"
      "   .set z, 128+32*(m/72)+4*c\n"
      "   .set b, 64+4*(2*c+1-n)\n"
      "   .if (m < 72) && (n == 0)\n"
      "    v_mfma_f32_16x16x32_bf16 v[z:z+3], v[0:3], v[b:b+3], 0\n"
      "   .else\n"
      "    v_mfma_f32_16x16x32_bf16 v[z:z+3], v[4*n:4*n+3], v[b:b+3], v[z:z+3]\n"
      "   .endif\n"
      "   .set cr, 7-r/4\n"
      "   .set cy, 7-y/4\n"
      "   .if (r < 32) && ((m >= 72) || (c < cr) || ((c == cr) && (n >= 2)))\n"
      "    .set b, 2*cr+1-(r&3)/2\n"
      "    ds_read_b64 v[64+4*b+2*(r&1):65+4*b+2*(r&1)], v[192+2*(b&1)+(r&1)] offset:128*(b/2)\n"
      "    .set r, r+1\n"
      "   .elseif (y < 32) && (m >= 80-cy*(cy+1))\n"
      "    .if y&3\n"
      "     v_mov_b32 v[160+4*cy+(y&3)], v[127+4*cy+(y&3)]\n"
      "    .else\n"
      "     v_mov_b32 v[160+4*cy], 0\n"
      "    .endif\n"
      "    .set y, y+1\n"
      "   .endif\n"
      "   .set m, m+1\n"
      "   .set n, n+1\n"
      "  .endr\n"
      "  .set c, c-1\n"
      " .endr\n"
      ".endr\n"
      "s_setprio 3\n"
      "s_waitcnt vmcnt(0)\n"
      "s_nop 5\n"
      : "=&{v[128:159]}"(z1), "=&{v[160:191]}"(z0), "+{v[64:95]}"(b0), "+{v[96:127]}"(b1), "+{v[224:255]}"(held)
      : "{v[0:31]}"(a0), "{v[32:63]}"(a1), "{v[192:195]}"(cp.ba), "v"(cp.fl), "v"(2 + 8 * round)
      : "memory");
  ST(10);
  const int lane = lane_id();
  lds_wait_at(cp.fl ^ 4, 2 + 8 * round);
  {
    const bool last = lane == 63;
    at<f32x4>(cp.cw) = f32x4{last ? 0.f : z1[3], last ? z1[3] : z1[7], last ? z1[7] : z1[11], last ? z1[11] : z1[15]};
    at<f32x4>(cp.cw + 1024) = f32x4{last ? z1[15] : z1[19], last ? z1[19] : z1[23], last ? z1[23] : z1[27], last ? z1[27] : z1[31]};
  }
  if (pw) conv_send<0>(cp, z0);
  else conv_send<1>(cp, z0);
  pair_sync(cp, 3 + 8 * round);
  if (pw) conv_keep<1>(cp, z0);
  else conv_keep<0>(cp, z0);
  int r2 = cp.yr - (lane & (32 * pw)) * 4, o2 = lane < 48 ? cp.yo + 2048 : OOB;
  asm volatile("" : "+v"(r2), "+v"(o2));
  pair_sync(cp, 4 + 8 * round);
  v2u ya[3], yb[3];
#pragma unroll
  for (int n = 0; n < 3; ++n) {
    LAS char* a = (LAS char*)(size_t)(n < 2 ? cp.yr : r2) + n * 768;
    ya[n] = trread(a);
    yb[n] = trread(a + 128);
  }
  SB();
#pragma unroll
  for (int n = 0; n < E_YST; ++n) st128<SC1>(v4u{ya[n].x, ya[n].y, yb[n].x, t32}, mk(p.ws, WS_END), n < 2 ? cp.yo + n * 1024 : o2);
  ST(12);
}

#define ASM_TS(n)
#define ASM_TC(n)
#define V10(a) "v" #a "0", "v" #a "1", "v" #a "2", "v" #a "3", "v" #a "4", "v" #a "5", "v" #a "6", "v" #a "7", "v" #a "8", "v" #a "9"
__device__ __forceinline__ int xs_at(int a, int lane) { return (lane & 15) * 128 + (((2 * a + (lane >> 5)) ^ (lane & 7)) << 4) + ((lane >> 4) & 1) * 8; }
#define F_NAP 1
#define F_PRIO 1
__device__ __forceinline__ void finish(const Params& p, LAS char* L, const v2u (&xv)[NT], int t0, int rng, int G2, int a, unsigned t32, int lane) {
  const int i16 = lane & 15, g = lane >> 4;
  f32x2 st[NT];
#pragma unroll
  for (int u = 0; u < NT; ++u) st[u] = *(LAS f32x2*)(L + L_STAT2 + (16 * u + i16) * 8);
  const v2u bv = *(LAS v2u*)(L + L_B2 + a * 32 + g * 8);
  SB();
  f32x32 x, s;
#pragma unroll
  for (int u = 0; u < NT; ++u) {
    x[4 * u] = blo(xv[u].x), x[4 * u + 1] = bhi(xv[u].x), x[4 * u + 2] = blo(xv[u].y), x[4 * u + 3] = bhi(xv[u].y);
#pragma unroll
    for (int r = 0; r < 4; ++r) {
      float d = x[4 * u + r] - st[u].x;
      asm("" : "+v"(d));
      d *= st[u].y;
      asm("" : "+v"(d));
      s[4 * u + r] = d;
    }
  }
  x[24] = blo(bv.x), x[25] = bhi(bv.x), x[26] = blo(bv.y), x[27] = bhi(bv.y);
  x[28] = x[30] = -1.442695041f, x[29] = x[31] = 1.f;
  const int zb = (int)(size_t)L + a * 1024, yb = WS_YT + (64 * G2 + 16 * a) * YS + rng * 256;
  s[24] = __int_as_float(zb + lane * 16), s[25] = s[26] = __int_as_float(zb + 65536 + lane * 16);
  s[27] = __int_as_float((int)(size_t)L + L_FLAG + (F_DONE + (lane & 3)) * 4);
  s[28] = __int_as_float(4 * g * YS + i16 * 16);
  s[29] = s[30] = s[31] = __int_as_float((16 * t0 + i16) * 1280 + G2 * 128 + a * 32 + g * 8);
  ST(13);
  int cnt, tmp;
  asm volatile(
      ".macro OP8 op, d, a, b, da, db\n"
      " .set e, 0\n .rept 8\n \\op v[\\d+e], v[\\a+(e&3)+\\da*(e/4)], v[\\b+(e&3)+\\db*(e/4)]\n .set e, e+1\n .endr\n"
      ".endm\n"
      ".macro PK4 op, d, a, b, da, db\n"
      " .set e, 0\n .rept 4\n .set a, \\a+(e&2)+\\da*(e/4)\n .set b, \\b+(e&2)+\\db*(e/4)\n"
      "  \\op v[\\d+e:\\d+e+1], v[a:a+1], v[b:b+1]\n .set e, e+2\n .endr\n"
      ".endm\n"
      ".macro TR8 op\n"
      " .set e, 0\n .rept 8\n \\op v[120+e], v[120+e]\n .set e, e+1\n .endr\n"
      ".endm\n"
      ".macro YLD\n"
      " buffer_load_dwordx4 v[96:99], v60, %4, %6 offen sc1\n buffer_load_dwordx4 v[100:103], v60, %4, %7 offen sc1\n"
      " buffer_load_dwordx4 v[104:107], v60, %4, %8 offen sc1\n buffer_load_dwordx4 v[108:111], v60, %4, %9 offen sc1\n"
      ".endm\n"
      ".macro PUT u, o\n"
      " v_cvt_pk_bf16_f32 v[\\o], v[\\o], v[\\o+1]\n v_cvt_pk_bf16_f32 v[\\o+1], v[\\o+2], v[\\o+3]\n"
      " s_mov_b32 %3, \\u*16*1280\n"
      " buffer_store_dwordx2 v[\\o:\\o+1], v61, %5, %3 offen\n"
      ".endm\n"
      ".macro ZDT P\n"
      " .set n, 0\n"
      " .rept 8\n"
      "  .set t, 2*\\P+((n/4)^(n&1))\n"
      "  ds_read_b128 v[64+4*n:67+4*n], v[56+t/4] offset:(t&3)*16384+(n&3)*4096\n"
      "  .set n, n+1\n"
      " .endr\n"
      ".endm\n"
      ".macro ZRD P\n"
      " s_mov_b32 %2, 0x400000\n"
      " 8:\n"
      " ds_read_b32 v128, v59 offset:16*\\P\n"
      " s_sub_u32 %2, %2, 1\n"
      " s_cbranch_scc1 9f\n"
      " .if \\P == 0\n s_sleep 1\n .endif\n"
      " s_waitcnt lgkmcnt(0)\n"
      " v_cmp_eq_u32 vcc, 0, v128\n"
      " s_cbranch_vccnz 8b\n"
      " 9:\n"
      " ZDT \\P\n"
      ".endm\n"
      "s_branch 4f\n"
      "1:\n"
      "YLD\n"
      "s_branch 2f\n"
      "4:\n"
      ".set P, 0\n"
      ".rept 2\n"
      " ZRD P\n"
      " .if P == 0\n YLD\n" ASM_TS(0) " .endif\n"
      " s_waitcnt lgkmcnt(0)\n"
      " OP8 v_add_f32, 112, 64, 68, 16, 16\n"
      " OP8 v_add_f32, 120, 72, 76, 16, 16\n"
      " OP8 v_add_f32, 112, 112, 24, 4, 0\n"
      " OP8 v_add_f32, 112, 112, 120, 4, 4\n"
      " .set e, 0\n .rept 8\n v_mul_f32 v[120+e], v28, v[112+e]\n .set e, e+1\n .endr\n"
      " TR8 v_exp_f32\n"
      " OP8 v_mul_f32, 32+8*P, 32+8*P, 112, 4, 4\n"
      " .set e, 0\n .rept 8\n v_add_f32 v[120+e], 1.0, v[120+e]\n .set e, e+1\n .endr\n"
      " TR8 v_rcp_f32\n"
      " s_nop 0\n"
      " OP8 v_mul_f32, 32+8*P, 32+8*P, 120, 4, 4\n"
      " .set P, P+1\n"
      ".endr\n" ASM_TS(1)
      "s_mov_b32 %2, 0x8000\n"
      "2:\n"
      "s_waitcnt vmcnt(0)\n"
      "v_xor_b32 v112, %10, v99\n v_xor_b32 v113, %10, v103\n v_xor_b32 v114, %10, v107\n v_xor_b32 v115, %10, v111\n"
      "v_or3_b32 v112, v112, v113, v114\n v_or_b32 v112, v112, v115\n"
      "s_sub_u32 %2, %2, 1\n"
      "v_cmp_ne_u32 vcc, 0, v112\n"
      "s_cbranch_scc1 3f\n"
      "s_cbranch_vccnz 1b\n"
      "3:\n" ASM_TS(2)
      "ds_read_b32 v128, v59 offset:32\n"
      "ZDT 2\n"
      ".set u, 5\n"
      ".rept 6\n"
      " .set o, 112+4*(u&3)\n"
      " .set r, 0\n"
      " .rept 4\n"
      "  .if u&1\n v_and_b32 v[o+r], 0xffff0000, v[96+4*r+u/2]\n .else\n v_lshlrev_b32 v[o+r], 16, v[96+4*r+u/2]\n .endif\n"
      "  .set r, r+1\n"
      " .endr\n"
      " .set r, 0\n"
      " .rept 4\n"
      "  .if u < 4\n v_fma_f32 v[o+r], v[o+r], v[32+4*u+r], v[4*u+r]\n .else\n v_mul_f32 v[32+4*u+r], v[o+r], v[32+4*u+r]\n .endif\n"
      "  .set r, r+1\n"
      " .endr\n"
      " .if u < 4\n PUT u, o\n .endif\n"
      " .set u, u-1\n"
      ".endr\n"
      "s_waitcnt lgkmcnt(0)\n"
      "v_cmp_eq_u32 vcc, 0, v128\n"
      "s_cbranch_vccz 5f\n"
      "ZRD 2\n"
      "s_waitcnt lgkmcnt(0)\n"
      "5:\n"
      "PK4 v_pk_add_f32, 112, 64, 68, 16, 16\n"
      "PK4 v_pk_add_f32, 120, 72, 76, 16, 16\n"
      "PK4 v_pk_add_f32, 112, 112, 24, 4, 0\n"
      "PK4 v_pk_add_f32, 112, 112, 120, 4, 4\n"
      ".set e, 0\n .rept 4\n v_pk_mul_f32 v[120+e:121+e], v[112+e:113+e], v[28:29] op_sel_hi:[1,0]\n .set e, e+2\n .endr\n"
      "TR8 v_exp_f32\n"
      "PK4 v_pk_mul_f32, 48, 48, 112, 4, 4\n"
      ".set e, 0\n .rept 4\n v_pk_add_f32 v[120+e:121+e], v[120+e:121+e], v[28:29] op_sel:[0,1]\n .set e, e+2\n .endr\n"
      "TR8 v_rcp_f32\n"
      "s_nop 0\n"
      ".set e, 0\n .rept 4\n v_pk_fma_f32 v[112+e:113+e], v[120+e:121+e], v[48+e:49+e], v[16+e:17+e]\n .set e, e+2\n .endr\n"
      "PUT 4, 112\n"
      "PUT 5, 116\n"
      ".purgem OP8\n .purgem PK4\n .purgem TR8\n .purgem YLD\n .purgem ZDT\n .purgem PUT\n .purgem ZRD\n"
      : "+{v[0:31]}"(x), "+{v[32:63]}"(s), "=&s"(cnt), "=&s"(tmp)
      : "s"(mkd(p.ws, WS_END)), "s"(mkd(p.out, S * D * 2)), "s"(yb), "s"(yb + YS), "s"(yb + 2 * YS), "s"(yb + 3 * YS), "s"(t32)
      : "memory", "scc", "vcc", "v64", "v65", "v66", "v67", "v68", "v69", V10(7), V10(8), V10(9), V10(10), V10(11), V10(12), V10(13)
  );
  ST(7);
  RAW(5, __builtin_amdgcn_readfirstlane(__float_as_int(x[0])));
  RAW(6, __builtin_amdgcn_readfirstlane(__float_as_int(x[1])));
  RAW(11, __builtin_amdgcn_readfirstlane(__float_as_int(x[2])));
}

#define WRN "2"
#ifndef M_PRIO
#define M_PRIO "2"
#endif
#define WB "2"
#define B9 "2"
#define HB "7"
#define HN "31"
__device__ __forceinline__ void mma_waves(const Params& p, LAS char* L, int i, int j, bool job, int t0, int G2, int rot, int w, int lane) {
  const int fh = w & 1, th = w >> 1, i16 = lane & 15, g = lane >> 4, q = (lane >> 2) & 3, Lb = (int)(size_t)L;
  const int fb = 128 * j + 64 * fh;
  v2u b1v[4];
#pragma unroll
  for (int m = 0; m < 4; ++m) b1v[m] = __builtin_amdgcn_raw_buffer_load_b64(mk(p.b1, F * 2), (fb + 16 * m + 4 * g) * 2, 0, 0);
  const v2u b2v = __builtin_amdgcn_raw_buffer_load_b64(mk(p.b2, D * 2), (64 * G2 + 16 * w + 4 * g) * 2, 0, 0);
  SB();
  const int sb = 27 * th + 13 * fh, ss = 26 + 27 * th;
  i32x16 va, vb;
#pragma unroll
  for (int m = 0; m < 4; ++m) va[m] = Lb + L_W1 + (8 * g + q) * 256 + (((4 * fh + m) ^ (q | ((g & 1) << 2))) << 5) + 8 * (lane & 3);
#pragma unroll
  for (int ks = 0; ks < 2; ++ks) va[4 + ks] = Lb + (64 * th + i16) * 128 + (((4 * ks + g) ^ (i16 >> 1)) << 4);
  va[6] = Lb + L_STAT + (64 * th + i16) * 8;
  va[7] = Lb + L_CS + (64 * fh + 4 * g) * 4;
  va[8] = Lb + L_FLAG + w * 4;
  va[9] = Lb + L_FLAG + (lane & 3) * 4;
  va[10] = lane * 16 + WS_HT + (4 * j + 2 * fh) * HS + (8 * i + 4 * th) * 1024;
  va[11] = va[10] + HS;
  va[14] = 1;
  {
    int so[4];
    w2_src(so, lane);
    va[15] = so[0];
  }
  {
    const int rr = 4 * g + q;
#pragma unroll
    for (int dd = 0; dd < 4; ++dd) {
      vb[dd] = Lb + w * 4096 + rr * 128 + ((dd ^ ((rr >> 1) & 3)) << 5) + 8 * (lane & 3);
      vb[4 + dd] = vb[dd] + 65536;
    }
    vb[8] = lane * 16 + fh * 1024;
    vb[9] = lane * 16 + 1024 - fh * 1024;
    vb[10] = vb[8] + 4096;
    vb[11] = vb[9] + 4096;
    vb[12] = Lb + w * 4096 + lane * 16;
    vb[13] = vb[12] + 65536;
    vb[14] = vb[15] = va[8];
    va[12] = lane == 0 ? flag_at(FH + (2 * i + th) * 32 + 2 * j + fh) : OOB;
    const int tl = min(t0 + 4 * (lane >> 3), t0 + NT - 1), c = lane & 7;
    va[13] = flag_at(FH + (tl >> 2) * 32 + (c == 7 ? ss >> 1 : (sb >> 1) + c));
  }
  f32x32 acc[2];
  asm volatile(
      ".set n, 0\n"
      ".rept 32\n"
      " v_mov_b64 v[n:n+1], 0\n"
      " .set n, n+2\n"
      ".endr\n"
      : "={v[0:31]}"(acc[0]), "={v[32:63]}"(acc[1]), "+{v[128:143]}"(va));
  i32x16 sh;
  i32x8 sw;
#pragma unroll
  for (int k = 0; k < 16; ++k)
    sh[k] = WS_HT + t0 * 1024 + g2_step(w, rot, min(k, 13)) * HS;
#pragma unroll
  for (int k = 0; k < 8; ++k) sw[k] = G2 * 128 + g2_step(w, rot, 5 + k) * 40960;
  int role = __builtin_amdgcn_readfirstlane((fb < F ? 1 : 0) + (job ? 2 : 0));
  asm volatile("" : "+{s[52:67]}"(sh), "+{s[68:75]}"(sw), "+s"(role));
  KvPre kp;
  kv_pre(kp, L + L_KV, L + L_KT, w, lane);
  BAR();
  kv_make<1>(kp);
  SB();
  f32x16 bb;
#pragma unroll
  for (int m = 0; m < 4; ++m) {
    bb[4 * m] = blo(b1v[m].x);
    bb[4 * m + 1] = bhi(b1v[m].x);
    bb[4 * m + 2] = blo(b1v[m].y);
    bb[4 * m + 3] = bhi(b1v[m].y);
  }
  *(LAS v2u*)(L + L_B2 + w * 32 + g * 8) = b2v;
  {
    int cnt, zo = 65536 - Lb - w * 4096;
    asm volatile(
        ".macro G1RD n, s, sl, ks\n"
        " .if (\\n %% 3) == 2\n"
        "  ds_read_b128 v[\\s+16+4*(\\n/3):\\s+19+4*(\\n/3)], v[132+\\ks] offset:\\sl*16384+(\\n/3)*2048\n"
        " .else\n"
        "  ds_read_b64_tr_b16 v[\\s+4*(\\n/3)+2*(\\n%%3):\\s+4*(\\n/3)+2*(\\n%%3)+1], v[128+\\n/3] offset:\\sl*16384+\\ks*8192+(\\n%%3)*1024\n"
        " .endif\n"
        ".endm\n"
        ".macro STRD n\n"
        " .if \\n == 0\n"
        "  ds_read_b32 v164, v137 offset:%c12*4\n"
        " .elseif \\n < 5\n"
        "  ds_read_b64 v[166+2*\\n:167+2*\\n], v134 offset:(\\n-1)*128\n"
        " .else\n"
        "  ds_read_b128 v[156+4*\\n:159+4*\\n], v135 offset:(\\n-5)*64\n"
        " .endif\n"
        ".endm\n"
        ".macro G1H c, s, sl, ks, st\n"
        " .set n, 0\n"
        " .rept 16\n"
        "  v_mfma_f32_16x16x32_bf16 v[4*n:4*n+3], v[\\c+4*(n&3):\\c+4*(n&3)+3], v[\\c+16+4*(n/4):\\c+19+4*(n/4)], v[4*n:4*n+3]\n"
        "  .if (n < 12) && (\\sl >= 0)\n"
        "   G1RD n, \\s, \\sl, \\ks\n"
        "  .endif\n"
        "  .if (n >= 4) && (n < 13) && \\st\n"
        "   .set m, n-4\n"
        "   STRD m\n"
        "  .endif\n"
        "  .set n, n+1\n"
        " .endr\n"
        ".endm\n"
        ".macro LWAIT a, f, v\n"
        " s_mov_b32 %7, 0x400000\n"
        " 8:\n"
        " ds_read_b32 v255, \\a offset:\\f*4\n"
        " s_sub_u32 %7, %7, 1\n"
        " s_cbranch_scc1 9f\n"
        " s_waitcnt lgkmcnt(0)\n"
        " v_cmp_gt_i32 vcc, \\v, v255\n"
        " s_cbranch_vccnz 8b\n"
        " 9:\n"
        ".endm\n"
        ".macro PK4 op, d, a, b, c, da, db, dc, mod:vararg\n"
        " .set e, 0\n"
        " .rept 4\n"
        "  .if \\c >= 0\n"
        "   \\op v[\\d+e:\\d+e+1], v[\\a+\\da*e:\\a+\\da*e+1], v[\\b+\\db*e:\\b+\\db*e+1], v[\\c+\\dc*e:\\c+\\dc*e+1] \\mod\n"
        "  .else\n"
        "   \\op v[\\d+e:\\d+e+1], v[\\a+\\da*e:\\a+\\da*e+1], v[\\b+\\db*e:\\b+\\db*e+1] \\mod\n"
        "  .endif\n"
        "  .set e, e+2\n"
        " .endr\n"
        ".endm\n"
        ".macro TR8 op\n"
        " .set e, 0\n"
        " .rept 8\n"
        "  \\op v[64+e], v[64+e]\n"
        "  .set e, e+1\n"
        " .endr\n"
        ".endm\n"
        ".macro HST y\n"
        " buffer_store_dwordx4 v[16*(\\y&3)+8*(\\y/4):16*(\\y&3)+8*(\\y/4)+3], v[138+\\y/4], %8, 0 offen offset:(\\y&3)*1024 sc1\n"
        ".endm\n"
        ".macro HSL n\n"
        " .if (y >= " HB ") && (yp < y) && (" HN " & \\n)\n"
        "  HST yp\n"
        "  .set yp, yp+1\n"
        " .endif\n"
        ".endm\n"
        ".macro PUBLISH n\n"
        " s_waitcnt vmcnt(\\n)\n"
        " buffer_store_dwordx2 v[68:69], v140, %8, 0 offen sc1\n"
        ".endm\n"
        ".macro G2RD k, n\n"
        " .set b, (0x63052417 >> (4*(\\k&7))) & 7\n"
        " .set ws, 144+16*(\\k%%6)\n"
        " ds_read_b64_tr_b16 v[ws+2*\\n:ws+2*\\n+1], v[240+4*(b/4)+\\n/2] offset:(b&3)*16384+(\\n&1)*2048\n"
        ".endm\n"
        ".macro G2RDS q0, cnt\n"
        " .set rq, \\q0\n"
        " .rept \\cnt\n"
        "  .set rk, rq/8\n"
        "  .set rn, rq&7\n"
        "  G2RD rk, rn\n"
        "  .set rq, rq+1\n"
        " .endr\n"
        ".endm\n"
        ".macro W2LD k\n"
        " s_add_u32 m0, %19, ((0x63052417 >> (4*(\\k&7))) & 7)*16384\n"
        " s_nop 0\n"
        " buffer_load_dwordx4 v143, %17, s[63+\\k] offen lds\n"
        " buffer_load_dwordx4 v72, %17, s[63+\\k] offen offset:1024 lds\n"
        " buffer_load_dwordx4 v73, %17, s[63+\\k] offen offset:2048 lds\n"
        " buffer_load_dwordx4 v74, %17, s[63+\\k] offen offset:3072 lds\n"
        ".endm\n"
        ".macro G2AT q\n"
        " .if \\q < 54\n"
        "  .set gk, \\q / 6\n"
        "  .set gu, \\q %% 6\n"
        " .else\n"
        "  .set gu, 2*((\\q-54)/9)\n"
        "  .set gk, 9+(\\q-54)%%9\n"
        "  .if gk > 12\n"
        "   .set gu, gu+1\n"
        "   .set gk, gk-4\n"
        "  .endif\n"
        " .endif\n"
        ".endm\n"
        ".macro G2LD q\n"
        " G2AT \\q\n"
        " buffer_load_dwordx4 v[96+4*(\\q%%12):99+4*(\\q%%12)], v[248+(gu&1)+2*(gu/4)], %8, s[52+gk] offen offset:(gu&2)*1024 sc1\n"
        ".endm\n"
        ".macro G2MF d\n"
        " .set ws, 144+16*(k%%6)+4*\\d\n"
        " .if k > 0\n"
        "  v_mfma_f32_16x16x32_bf16 v[16*u+4*\\d:16*u+4*\\d+3], v[ws:ws+3], v[96+4*(q%%12):99+4*(q%%12)], v[16*u+4*\\d:16*u+4*\\d+3]\n"
        " .else\n"
        "  v_mfma_f32_16x16x32_bf16 v[16*u+4*\\d:16*u+4*\\d+3], v[ws:ws+3], v[96+4*q:99+4*q], 0\n"
        " .endif\n"
        ".endm\n"
        ".macro G2WR n\n"
        " .set dw, " WRN "*(k-9)+\\n/2-2+" WRN "\n"
        " .if (k >= 9) && (u > 0) && (\\n&1) && (dw >= " WRN "*(k-9)) && (dw < 4)\n"
        "  .set t, u-1\n"
        "  ds_write_b128 v[252+t/4], v[16*t+4*dw:16*t+4*dw+3] offset:(t&3)*16384+dw*1024\n"
        "  .if (t&1) && (dw == 3)\n"
        "   ds_write_b32 v254, v254 offset:(%c14+4*(t/2))*4\n"
        "  .endif\n"
        " .endif\n"
        ".endm\n"
        "s_branch 20f\n"
        "3:\n"
        "LWAIT v137, %c12, 1\n"
        ".set m, 1\n"
        ".rept 8\n"
        " STRD m\n"
        " .set m, m+1\n"
        ".endr\n"
        "s_waitcnt lgkmcnt(0)\n"
        "s_branch 4f\n"
        "5:\n"
        "s_barrier\n"
        "s_branch 6f\n"
        "10:\n"
        "PUBLISH 0\n"
        "s_branch 7f\n"
        "11:\n"
        "LWAIT v254, 4, 7\n"
        "s_branch 12f\n"
        "20:\n"
        "v_mov_b32 v160, 0xbdd2d3e8\n"
        "v_mov_b32 v161, 0xc0135761\n"
        "v_mov_b32 v162, 1.0\n"
        "v_and_b32 v163, -16, v136\n"
        "s_mov_b32 %7, 2\n"
        "s_waitcnt lgkmcnt(0)\n"
        "s_barrier\n" ASM_TS(0) ASM_TC(8)
        "ds_read_b64 v[166:167], v163 offset:%c10\n"
        ".set n, 0\n"
        ".rept 12\n"
        " G1RD n, 64, 0, 0\n"
        " .set n, n+1\n"
        ".endr\n"
        "1:\n"
        "s_waitcnt lgkmcnt(0)\n"
        "G1H 64, 96, 0, 1, 0\n"
        "s_waitcnt lgkmcnt(0)\n"
        "s_barrier\n"
        "G1H 96, 64, 1, 0, 0\n"
        "s_waitcnt lgkmcnt(0)\n"
        "G1H 64, 96, 1, 1, 0\n"
        "s_waitcnt lgkmcnt(0)\n"
        "s_barrier\n"
        "G1H 96, 64, 2, 0, 0\n"
        "s_waitcnt lgkmcnt(0)\n"
        "G1H 64, 96, 2, 1, 0\n"
        "s_waitcnt lgkmcnt(0)\n"
        "s_barrier\n"
        "s_sub_u32 %7, %7, 1\n"
        "G1H 96, 64, 0, 0, 0\n"
        "s_cbranch_scc0 1b\n"
        "s_waitcnt lgkmcnt(0)\n"
        "G1H 64, 96, 0, 1, 1\n"
        "s_waitcnt lgkmcnt(0)\n"
        "v_cmp_eq_u32 vcc, 0, v164\n"
        "s_bitcmp1_b32 %11, 0\n"
        "G1H 96, 64, -1, 0, 0\n"
        "s_setprio 2\n" ASM_TS(1) ASM_TC(9)
        "s_cbranch_scc0 5b\n"
        "s_cbranch_vccnz 3b\n"
        "4:\n" ASM_TS(2)
        ".set y, 0\n"
        ".set yp, 0\n"
        ".rept 8\n"
        " .set a, 16*(y&3)+8*(y/4)\n"
        " .set t, 168+2*(y&3)\n"
        " PK4 v_pk_fma_f32, a, t, 176+8*(y/4), a, 0, 1, 1, op_sel_hi:[0,1,1] neg_lo:[1,0,0] neg_hi:[1,0,0]\n"
        " HSL 1\n"
        " PK4 v_pk_fma_f32, a, a, t, 144+8*(y/4), 1, 0, 1, op_sel:[0,1,0]\n"
        " .if y == " B9 "\n"
        "  s_barrier\n"
        " .endif\n"
        " PK4 v_pk_mul_f32, 64, a, a, -1, 1, 1, 0\n"
        " PK4 v_pk_fma_f32, 64, 64, 160, 160, 1, 0, 0, op_sel:[0,0,1] op_sel_hi:[1,0,1]\n"
        " PK4 v_pk_mul_f32, 64, a, 64, -1, 1, 1, 0\n"
        " HSL 2\n"
        " TR8 v_exp_f32\n"
        " HSL 4\n"
        " PK4 v_pk_add_f32, 64, 64, 162, -1, 1, 0, 0, op_sel_hi:[1,0]\n"
        " HSL 8\n"
        " TR8 v_rcp_f32\n"
        " HSL 16\n"
        " PK4 v_pk_mul_f32, a, a, 64, -1, 1, 1, 0\n"
        " v_cvt_pk_bf16_f32 v[a], v[a], v[a+1]\n"
        " v_cvt_pk_bf16_f32 v[a+1], v[a+2], v[a+3]\n"
        " v_cvt_pk_bf16_f32 v[a+2], v[a+4], v[a+5]\n"
        " v_cvt_pk_bf16_f32 v[a+3], v[a+6], v[a+7]\n"
        " .set y, y+1\n"
        ".endr\n"
        ".rept 8-yp\n"
        " HST yp\n"
        " .set yp, yp+1\n"
        ".endr\n"
        "6:\n" ASM_TS(3)
        "v_mov_b64 v[68:69], v[166:167]\n"
        "s_bitcmp1_b32 %11, 1\n"
        "v_add_u32 v72, 9216, v143\n"
        "v_add_u32 v73, 2*9216, v143\n"
        "v_add_u32 v74, 3*9216, v143\n"
        "s_barrier\n"
        "s_cbranch_scc0 10b\n"
        ".set k, 5\n"
        ".rept " WB "\n"
        " W2LD k\n"
        " .set k, k+1\n"
        ".endr\n"
        "G2RDS 0, 40\n"
        "PUBLISH 4*" WB "\n" ASM_TS(4)
        ".rept 3-" WB "\n"
        " W2LD k\n"
        " .set k, k+1\n"
        ".endr\n"
        "s_waitcnt lgkmcnt(0)\n"
        "W2LD 8\n"
        "W2LD 9\n"
        "W2LD 10\n"
        "W2LD 11\n" ASM_TS(5)
        "s_mov_b32 %7, 0x8000\n"
        "8:\n"
        "buffer_load_dwordx2 v[70:71], v141, %8, 0 offen sc1\n"
        "s_sub_u32 %7, %7, 1\n"
        "s_cbranch_scc1 9f\n"
        "s_waitcnt vmcnt(0)\n"
        "v_cmp_ne_u64 vcc, v[68:69], v[70:71]\n"
        "s_mov_b64 exec, vcc\n"
        "s_cbranch_vccnz 8b\n"
        "9:\n"
        "s_mov_b64 exec, -1\n" ASM_TS(6)
        ".set q, 0\n"
        ".rept 11\n"
        " G2LD q\n"
        " .set q, q+1\n"
        ".endr\n"
        "s_setprio 0\n"
        ".set q, 0\n"
        ".rept 81\n"
        " G2AT q\n"
        " .set k, gk\n"
        " .set u, gu\n"
        " .set o, 10\n"
        " .if q > 70\n"
        "  .set o, 80-q\n"
        " .endif\n"
        " .if ((q%%6) == 0) && (q >= 6) && (q <= 54)\n"
        "  s_waitcnt vmcnt(o) lgkmcnt(0)\n"
        " .else\n"
        "  s_waitcnt vmcnt(o)\n"
        " .endif\n"
        " G2MF 0\n"
        " .set ql, q+11\n"
        " .if ql < 81\n"
        "  G2LD ql\n"
        " .endif\n"
        " G2WR 0\n"
        " G2MF 1\n"
        " .if q == 6\n"
        "  ds_write_b32 v254, v254 offset:%c13*4\n"
        " .endif\n"
        " .if q == 41\n"
        "  ds_read_b32 v255, v254 offset:16\n"
        " .endif\n"
        " .if q == 42\n"
        "  v_cmp_gt_i32 vcc, 7, v255\n"
        " .endif\n"
        " G2WR 1\n"
        " G2MF 2\n"
        " .set kr, k+5\n"
        " .if (u >= 1) && (u <= 4) && (k <= 8)\n"
        "  .set n, 2*u-2\n"
        "  G2RD kr, n\n"
        " .endif\n"
        " G2WR 2\n"
        " G2MF 3\n"
        " .if (u >= 1) && (u <= 4) && (k <= 8)\n"
        "  .set n, 2*u-1\n"
        "  G2RD kr, n\n"
        " .endif\n"
        " G2WR 3\n"
        " .if q == 42\n"
        "  s_cbranch_vccnz 11b\n"
        "  12:\n"
        "  s_setprio " M_PRIO "\n"
        " .endif\n"
        " .set q, q+1\n"
        ".endr\n"
        "s_nop 4\n"
        ".set n, 0\n .rept 4\n ds_write_b128 v253, v[80+4*n:83+4*n] offset:16384+n*1024\n .set n, n+1\n .endr\n"
        "ds_write_b32 v254, v254 offset:(%c14+8)*4\n"
        "7:\n"
        ".purgem G1RD\n .purgem STRD\n .purgem G1H\n .purgem LWAIT\n .purgem PK4\n .purgem TR8\n .purgem HST\n .purgem HSL\n .purgem PUBLISH\n"
        ".purgem G2RD\n .purgem G2RDS\n .purgem G2AT\n .purgem G2WR\n .purgem W2LD\n .purgem G2LD\n .purgem G2MF\n"
        : "+{v[0:31]}"(acc[0]), "+{v[32:63]}"(acc[1]), "+s"(zo), "+{v[128:143]}"(va), "+{v[144:159]}"(bb), "+{v[240:255]}"(vb), "+{s[52:67]}"(sh), "=&s"(cnt)
        : "s"(mkd(p.ws, WS_END)), "n"(0), "n"(L_TAG - L_FLAG), "s"(role), "n"(F_STAT), "n"(F_GELU), "n"(F_DONE), "n"(0), "n"(HS),
          "s"(mkd(p.w2, F * D * 2)), "{s[68:75]}"(sw), "s"(Lb + w * 4096)
        : "memory", "scc", "vcc", "v64", "v65", "v66", "v67", "v68", "v69", V10(7), V10(8), V10(9), V10(10), V10(11), "v120", "v121", "v122", "v123", "v124", "v125", "v126", "v127",
          V10(16), V10(17), V10(18), V10(19), V10(20), V10(21), V10(22), V10(23)
    );
  }
  if (job) ST(5);
}

constexpr int P_KT = 6 * CONV_BUF, P_KV = P_KT + 24576;
static_assert(P_KV + 3 * KV_IN <= L_FLAG, "lds");
__device__ __forceinline__ float dot2(unsigned a, unsigned b, float c) {
  return __builtin_amdgcn_fdot2_f32_bf16(__builtin_bit_cast(bf2_t, a), __builtin_bit_cast(bf2_t, b), c, false);
}
__device__ __forceinline__ float sum8(float v) {
  v += __int_as_float(__builtin_amdgcn_mov_dpp(__float_as_int(v), 0xb1, 0xf, 0xf, true));
  v += __int_as_float(__builtin_amdgcn_mov_dpp(__float_as_int(v), 0x4e, 0xf, 0xf, true));
  return v + __int_as_float(__builtin_amdgcn_mov_dpp(__float_as_int(v), 0x141, 0xf, 0xf, true));
}
__device__ __forceinline__ void prep_load(const Params& p, LAS char* L, int pp, int w, int lane) {
  const int row = lane >> 3, o = (64 * pp + 32 * (w & 1) + row) * 1280 + 7 * 128 + (((lane & 7) ^ ((row >> 1) & 2)) << 4);
  const int vo[4] = {o ^ 64, o + 9216, (o ^ 64) + 2 * 9216, o + 3 * 9216};
  const v4u rk = mkd(w >= 2 ? p.v : p.k, S * D * 2);
#pragma unroll
  for (int n = 0; n < 3; ++n) {
    dma4(rk, L + P_KV + n * KV_IN + 1024 + w * 4096, vo, n * 128);
    if (w == 0) dma16(rk, L + P_KV + n * KV_IN, row >= 5 ? o - 8 * 1280 : OOB, n * 128);
    if (w == 3) dma16(mkd(p.sk, D * 8), L + P_KV + n * KV_IN + 17408, lane < 32 ? lane * 16 : OOB, (7 + n) * 512);
  }
  __builtin_amdgcn_s_barrier();
}
__device__ __forceinline__ unsigned prep_kv(const Params& p, LAS char* L, int pp, int w, int lane) {
  const int c0 = 448 + 6 * pp;
  u64 ep = ask_epoch(p);
  TS(0);
  KvPre kp;
  kv_pre(kp, L + P_KV, L + P_KT, w, lane);
  TS(6);
  asm volatile("s_waitcnt vmcnt(0) lgkmcnt(0)" ::: "memory");
  lds_post(L, F_GELU + w, 1);
  lds_wait4(L, F_GELU, lane);
  TS(7);
  asm volatile("" : "+s"(ep));
  const u64 tag = tag_of(ep);
  kv_make<3>(kp);
  TS(1);
#pragma unroll
  for (int n = 0; n < 3; ++n) kv_store<2>(p, L + P_KT + n * 8192, 2 * w, pp, 7 + n, lane);
  lds_post(L, F_RING + w, 1);
  TS(2);
  WAIT_V(0);
  set_flag(p, lane < 3 ? flag_at(FK + (28 + 4 * lane + w) * 32 + pp) : OOB, tag);
  TS(3);
  if (w == 3) {
    wait_flag<2>(p, flag_at(FK + ((c0 + (lane < 32 ? 0 : 5)) >> 4) * 32 + (lane & 31)), tag);
    lds_post(L, F_STAT, 1);
  }
  lds_wait<1>(L, F_STAT, 1);
  TS(4);
  stage_u(p, L + w * CONV_BUF, u_src(c0 + w, lane));
  if (w < 2) stage_u(p, L + (4 + w) * CONV_BUF, u_src(c0 + 4 + w, lane));
  WAIT_V(0);
  lds_post(L, F_STAGED + w, 1);
  TS(5);
  return tag >> 32;
}
__device__ __forceinline__ unsigned prep_side(const Params& p, LAS char* L, int pp, int a, int lane) {
  const int c0 = 448 + 6 * pp + 3 * (a & 1);
  LAS char* Lc = L + 3 * (a & 1) * CONV_BUF;
  u64 ep;
  if (a < 2) {
#pragma unroll
    for (int n = 0; n < 3; ++n) stage_pads(Lc + n * CONV_BUF, lane);
    u64 dd;
    v4u hh[3];
    asm volatile(
        "s_load_dwordx2 %0, %5, 0x0\n s_load_dwordx2 %1, %6, 0x0\n s_load_dwordx4 %2, %7, 0x0\n s_load_dwordx4 %3, %7, 0x1000\n"
        "s_load_dwordx4 %4, %7, 0x2000\n s_waitcnt lgkmcnt(0)"
        : "=&s"(ep), "=&s"(dd), "=&s"(hh[0]), "=&s"(hh[1]), "=&s"(hh[2])
        : "s"(p.ws + WS_EPOCH), "s"(p.dv + (c0 & ~1) * 2), "s"(p.hk + c0 * 4096)
        : "memory");
    dd >>= 16 * a;
#pragma unroll
    for (int c = 0; c < 3; ++c) stage_head(Lc + c * CONV_BUF, hh[c], ((unsigned)(dd >> (16 * c)) & 0xffffu) << 16);
  } else {
    *(LAS int*)(L + L_FLAG + lane * 4) = 0;
    ep = ask_epoch(p);
  }
  BAR();
  asm volatile("" : "+s"(ep));
  const u64 tag = tag_of(ep);
  {
    const rsrc_t rx = mk(p.x, S * D * 2);
    const int row = lane >> 3, c = lane & 7;
    v4u xr[20];
#pragma unroll
    for (int n = 0; n < 20; ++n) xr[n] = __builtin_amdgcn_raw_buffer_load_b128(rx, (64 * pp + 16 * a + row) * 1280 + c * 16 + (n % 10) * 128, (n / 10) * 8 * 1280, 0);
    SB();
    float sx[2], sq[2];
#pragma unroll
    for (int h = 0; h < 2; ++h) {
      sx[h] = sq[h] = 0.f;
#pragma unroll
      for (int n = 0; n < 40; ++n) {
        const unsigned v = xr[10 * h + (n >> 2)][n & 3];
        sx[h] = dot2(v, 0x3f803f80u, sx[h]);
        sq[h] = dot2(v, v, sq[h]);
      }
      sx[h] = sum8(sx[h]);
      sq[h] = sum8(sq[h]);
    }
    const float mean = (c ? sx[1] : sx[0]) * (1.f / D);
    const float var = fmaxf((c ? sq[1] : sq[0]) * (1.f / D) - mean * mean, 0.f);
    const unsigned md = pk(mean, __builtin_amdgcn_sqrtf(rb(rb(var) + 1e-5f)));
    __builtin_amdgcn_raw_buffer_store_b64(v2u{md, (unsigned)(tag >> 32)}, mk(p.ws, WS_END), c < 2 ? WS_STAT + (64 * pp + 16 * a + 8 * c + row) * 8 : OOB, 0, SC1);
    SB();
  }
  TS(0);
  lds_wait4<1>(L, F_RING, lane);
  if (a < 2) {
#pragma unroll
    for (int n = 0; n < 3; ++n) stage_h<0>(p, Lc + n * CONV_BUF, (c0 + n) * 4096 + lane * 16);
    WAIT_V(0);
#pragma unroll
    for (int n = 0; n < 3; ++n) stage_h<1>(p, Lc + n * CONV_BUF, (c0 + n) * 4096 + lane * 16);
    WAIT_V(0);
    lds_post(L, F_STAGED + 4 + a, 1);
  } else {
    touch(p.w2 + (448 * 32 + (2 * pp + a - 2) * 46 + min(lane, 45)) * 128, L + L_DUMP);
  }
  TS(2);
  return tag >> 32;
}

__device__ __forceinline__ void next_epoch(const Params& p) {
  const u64 tag = get_tag(p);
  const int lane = lane_id();
#pragma unroll 1
  for (int spin = 0; spin < SPIN_CAP; ++spin) {
    bool ok = true;
#pragma unroll
    for (int n = 0; n < 4; ++n) {
      const v2u f = __builtin_amdgcn_raw_buffer_load_b64(mk(p.ws, WS_END), flag_at(FK + (lane >> 5) * 128 + (lane & 31)), n * 256 * FSTR, SC1);
      ok &= f.y == (unsigned)(tag >> 32) && f.x == (unsigned)tag;
    }
    if (__all(ok)) break;
    asm volatile("s_sleep 8" ::: "memory");
  }
  set_flag(p, lane == 0 ? WS_EPOCH : OOB, tag);
}

__global__ __launch_bounds__(512, 2) void h3_kernel(Params p) {
  extern __shared__ __attribute__((aligned(16))) char smem[];
  LAS char* L = (LAS char*)smem;
  int lane = threadIdx.x & 63;
  const int w = __builtin_amdgcn_readfirstlane(threadIdx.x >> 6);
  const unsigned bid = blockIdx.x, xcd = bid & 7, r = bid >> 3;
  if (w >= 6) code_prefetch(__builtin_amdgcn_s_getpc() & ~255ull, L + L_DUMP, r + 32 * (w - 6), lane);
  SB();
  const bool g1 = r < 28;
  const int jb = 28 * xcd + r, jx = 28 * (xcd >> 1) + r, rng = (jx * 205) >> 10, g5 = jx - 5 * rng, G2 = 5 * (xcd & 1) + g5, t0 = min(6 * rng, 122);
  const bool job = jx < 110;
  const int rot = (ROT >> (4 * g5)) & 15;
  const int rr = (r * 37) >> 8, i = 4 * (xcd >> 1) + rr, j = 7 * (xcd & 1) + (r - 7 * rr);
  if (__builtin_expect(g1 && w < 4, 0)) {
    mma_waves(p, L, i, j, job, t0, G2, rot, w, lane);
    if (bid == 223 && w == 0) next_epoch(p);
    DUMP();
    __builtin_amdgcn_endpgm();
  }
  const int pp = 4 * xcd + (r - 28);
  __builtin_amdgcn_s_setprio(3);
  LoadPre lp;
  if (__builtin_expect(g1 && w < 6, 1)) gemm1_first(p, L, lp, i, j, jb, w - 4, lane);
  else if (w < 4) prep_load(p, L, pp, w, lane);
  lane = lane_id();
  ConvPre cp;
  conv_pre(cp, L, L + (g1 ? L_CONV - 2 * CONV_BUF : 0) + (w >> 1) * CONV_BUF, (g1 ? 2 * jb - 2 : 448 + 6 * pp) + (w >> 1), w, lane);
  int so[4], xa = (int)(size_t)L + L_XS + xs_at(w & 3, lane);
  w2_src(so, lane);
  int s12 = g2_step(w & 3, rot, 12) * 40960 + G2 * 128, s13 = g2_step(w & 3, rot, 13) * 40960 + G2 * 128;
  asm volatile("" : "+v"(so[0]), "+v"(so[1]), "+v"(so[2]), "+v"(so[3]), "+v"(xa), "+s"(s12), "+s"(s13));
  unsigned t32;
  v2u xv[NT];
  u32x32 held;
  const v4u rw2 = mkd(p.w2, job ? F * D * 2 : 0);
  if (__builtin_expect(g1, 1)) {
    if (__builtin_expect(w < 6, 1)) t32 = gemm1_load(p, L, lp, rw2, so, t0, G2, rot, jb, w - 4, lane);
    else t32 = gemm1_side(p, L, i, j, t0, jb, w - 6, lane);
    asm volatile(
        ".set n, 0\n .rept 4\n buffer_load_dwordx4 v[224+4*n:227+4*n], %1, %2, %3 offen\n buffer_load_dwordx4 v[240+4*n:243+4*n], %1, %2, %4 offen\n"
        " .if n < 3\n v_add_u32 %1, 8*1280, %1\n .endif\n .set n, n+1\n .endr\n"
        "s_setprio %5\n"
        : "=&{v[224:255]}"(held), "+v"(so[0])
        : "s"(rw2), "s"(s12), "s"(s13), "n"(H_PRIO)
        : "memory");
    TS(7);
#pragma unroll
    for (int u = 0; u < NT; ++u) xv[u] = at<v2u>(xa + u * 2048);
  } else {
    if (w < 4) __builtin_amdgcn_s_setprio(2);
    if (w < 4) t32 = prep_kv(p, L, pp, w, lane);
    else t32 = prep_side(p, L, pp, w - 4, lane);
    lds_wait<1>(L, F_STAGED + min(lane & 7, 5), 1);
    asm volatile("" : "={v[224:255]}"(held));
  }
#pragma unroll 1
  for (int round = 0;; ++round) {
    conv(p, cp, w, round, g1, held, t32);
    if (__builtin_expect(w >= 4 || round, 1)) break;
    cp.ba += 4 * CONV_BUF;
    cp.aa += 4 * CONV_BUF, cp.lb += 4 * CONV_BUF, cp.cw += 4 * CONV_BUF, cp.kw += 4 * CONV_BUF, cp.yr += 4 * CONV_BUF;
    cp.yo += 4 * YS;
    asm volatile("" : "+v"(cp.ba), "+v"(cp.aa), "+v"(cp.lb), "+v"(cp.cw), "+v"(cp.kw), "+v"(cp.yr), "+v"(cp.yo));
  }
  ST(4);
  if (g1 && job) {
    lane = lane_id();
    lds_wait_at<1, (F_GELU - 4) * 4>(cp.fl, 1);
#pragma unroll
    for (int n = 0; n < 8; ++n)
      *(LAS v4u*)(L + w2_block(12 + n / 4) * 16384 + (w - 4) * 4096 + (n & 3) * 1024 + lane * 16) = v4u{held[4 * n], held[4 * n + 1], held[4 * n + 2], held[4 * n + 3]};
    asm volatile("" ::: "memory");
    at<volatile int>(cp.fl) = 7;
    __builtin_amdgcn_s_setprio(F_PRIO);
    finish(p, L, xv, t0, rng, G2, w - 4, t32, lane);
  }
  DUMP();
}
}

extern "C" void kernel_launch(void* const* d_in, const int* in_sizes, int n_in, void* d_out, int out_size, void* d_ws, size_t ws_size,
                              hipStream_t stream) {
  using namespace h3;
  Params p = {};
  p.x = (const char*)d_in[0];
  p.k = (const char*)d_in[1];
  p.v = (const char*)d_in[2];
  p.sk = (const char*)d_in[3];
  p.hk = (const char*)d_in[4];
  p.dv = (const char*)d_in[5];
  p.w1 = (const char*)d_in[7];
  p.b1 = (const char*)d_in[8];
  p.w2 = (const char*)d_in[9];
  p.b2 = (const char*)d_in[10];
  p.out = (char*)d_out;
  p.ws = (char*)d_ws;
  (void)hipFuncSetAttribute((const void*)h3_kernel, hipFuncAttributeMaxDynamicSharedMemorySize, LDS_BYTES);
  h3_kernel<<<256, 512, LDS_BYTES, stream>>>(p);
}
```

```cpp
#include <hip/hip_runtime.h>
#include <stdint.h>

namespace h3 {
#define LAS __attribute__((address_space(3)))
#define GAS __attribute__((address_space(1)))
typedef unsigned v4u __attribute__((ext_vector_type(4)));
typedef unsigned v2u __attribute__((ext_vector_type(2)));
typedef float f32x4 __attribute__((ext_vector_type(4)));
typedef float f32x2 __attribute__((ext_vector_type(2)));
typedef float f32x16 __attribute__((ext_vector_type(16)));
typedef float f32x32 __attribute__((ext_vector_type(32)));
typedef unsigned u32x32 __attribute__((ext_vector_type(32)));
typedef int i32x16 __attribute__((ext_vector_type(16)));
typedef int i32x8 __attribute__((ext_vector_type(8)));
typedef short s16x8 __attribute__((ext_vector_type(8)));
typedef short s16x4 __attribute__((ext_vector_type(4)));
typedef __bf16 bf2_t __attribute__((ext_vector_type(2)));
typedef unsigned long long u64;
typedef __amdgpu_buffer_rsrc_t rsrc_t;

constexpr int S = 2048, D = 640, F = 1728, KS2 = 54, NT = 6;
constexpr int SC1 = 16;
#define LAYOUT5
#define LAYOUT11
#define LAYOUT15
constexpr int WS_EPOCH = 0;
constexpr int WS_STAT = 4096;
constexpr int WS_CS = 20480;
constexpr int HS = 129 * 1024;
constexpr int WS_HT = 196608;
constexpr int WS_KVT = WS_HT + KS2 * HS;
constexpr int YS = 22 * 256;
constexpr int WS_YT = WS_KVT + D * S * 2;
constexpr int WS_FL = WS_YT + D * YS;
constexpr int FSTR = 128;
constexpr int FH = 0;
constexpr int FK = 1024;
constexpr int WS_END = WS_FL + 2304 * FSTR;
__device__ __forceinline__ int flag_at(int n) { return WS_FL + n * FSTR; }
constexpr int WS_STAMP = 32 << 20;
constexpr int OOB = 0x40000000;
constexpr int L_W1 = 49152;
constexpr int L_KV = 98304, L_KT = L_KV + 18432;
constexpr int L_XS = 98304;
constexpr int C_A2 = 4128, C_B = 8256, C_BS = 1184, CONV_BUF = C_B + 4 * C_BS;
constexpr int L_CONV = 131072;
constexpr int L_STAT = L_CONV + 2 * CONV_BUF, L_CS = L_STAT + 1024, L_STAT2 = L_CS + 512, L_FLAG = L_STAT2 + 768, L_DUMP = L_FLAG + 256, L_TAG = L_DUMP + 1024, L_B2 = L_TAG + 16;
constexpr int LDS_BYTES = 163840;
static_assert(L_DUMP + 2048 <= LDS_BYTES, "lds");
constexpr int F_PAIR = 0, F_STAT = 12, F_GELU = 16, F_RING = 20, F_STAGED = 24, F_DONE = 32;
#define CODE_BYTES 32512
constexpr int SPIN_CAP = 1 << 15;

struct Params {
  const char *x, *k, *v, *sk, *hk, *dv, *w1, *b1, *w2, *b2;
  char *out, *ws;
};

__device__ __forceinline__ rsrc_t mk(const void* p, int bytes) { return __builtin_amdgcn_make_buffer_rsrc((void*)p, 0, bytes, 0x00020000); }
__device__ __forceinline__ f32x4 mfma(v4u a, v4u b, f32x4 c) {
  return __builtin_amdgcn_mfma_f32_16x16x32_bf16(__builtin_bit_cast(s16x8, a), __builtin_bit_cast(s16x8, b), c, 0, 0, 0);
}
__device__ __forceinline__ v2u trread(LAS char* a) { return __builtin_bit_cast(v2u, __builtin_amdgcn_ds_read_tr16_b64_v4i16((LAS s16x4*)a)); }
__device__ __forceinline__ v4u cat(v2u a, v2u b) { return v4u{a.x, a.y, b.x, b.y}; }
__device__ __forceinline__ float blo(unsigned u) { return __uint_as_float(u << 16); }
__device__ __forceinline__ float bhi(unsigned u) { return __uint_as_float(u & 0xffff0000u); }
__device__ __forceinline__ unsigned pk(float a, float b) {
  bf2_t r;
  r[0] = (__bf16)a;
  r[1] = (__bf16)b;
  return __builtin_bit_cast(unsigned, r);
}
__device__ __forceinline__ float rb(float a) { return blo(pk(a, a)); }
__device__ __forceinline__ unsigned pks(float a, float b) {
  asm("" : "+v"(a));
  asm("" : "+v"(b));
  return pk(a, b);
}
__device__ __forceinline__ v4u mkd(const void* p, int bytes) {
  const u64 a = (u64)p;
  return v4u{(unsigned)a, (unsigned)(a >> 32) & 0xffffu, (unsigned)bytes, 0x00020000u};
}
__device__ __forceinline__ void dma16(v4u rs, LAS char* dst, int voff, int soff) {
  asm volatile("s_mov_b32 m0, %0\n\ts_nop 0\n\tbuffer_load_dwordx4 %1, %2, %3 offen lds" ::"s"((unsigned)(size_t)dst), "v"(voff), "s"(rs), "s"(soff)
               : "memory");
}
__device__ __forceinline__ void dma16c(v4u rs, LAS char* dst, int voff, int soff) {
  asm volatile("s_mov_b32 m0, %0\n\ts_nop 0\n\tbuffer_load_dwordx4 %1, %2, %3 offen sc1 lds" ::"s"((unsigned)(size_t)dst), "v"(voff), "s"(rs), "s"(soff)
               : "memory");
}
__device__ __forceinline__ void dma4(v4u rs, LAS char* dst, const int (&vo)[4], int soff) {
  asm volatile(
      "s_mov_b32 m0, %0\n\ts_nop 0\n\tbuffer_load_dwordx4 %1, %5, %6 offen lds\n\tbuffer_load_dwordx4 %2, %5, %6 offen offset:1024 lds\n\t"
      "buffer_load_dwordx4 %3, %5, %6 offen offset:2048 lds\n\tbuffer_load_dwordx4 %4, %5, %6 offen offset:3072 lds" ::"s"((unsigned)(size_t)dst),
      "v"(vo[0]), "v"(vo[1]), "v"(vo[2]), "v"(vo[3]), "s"(rs), "s"(soff)
      : "memory");
}
template <int AUX>
__device__ __forceinline__ void st128(v4u v, rsrc_t rs, int voff) { __builtin_amdgcn_raw_buffer_store_b128(v, rs, voff, 0, AUX); }
#define WAIT_V(n) asm volatile("s_waitcnt vmcnt(" #n ")" ::: "memory")
#define WAIT_L0() asm volatile("s_waitcnt lgkmcnt(0)" ::: "memory")
#define BAR()                          \
  do {                                 \
    WAIT_L0();                         \
    __builtin_amdgcn_s_barrier();      \
    asm volatile("" ::: "memory");     \
  } while (0)
#define SB() __builtin_amdgcn_sched_barrier(0)
#define CLK "s_memrealtime"
#define RAW(idx, v)
#define ST(idx)
#define TS(k)
#define DUMP()

__device__ __forceinline__ int lane_id() {
  int l;
  asm volatile("v_mbcnt_lo_u32_b32 %0, -1, 0\n\tv_mbcnt_hi_u32_b32 %0, -1, %0" : "=v"(l));
  return l;
}
template <int NAP = 0>
__device__ __forceinline__ void wait_flag(const Params& p, int off, u64 tag) {
  int cnt = SPIN_CAP;
  u64 f;
  asm volatile(
      "1:\n"
      "buffer_load_dwordx2 %0, %2, %3, 0 offen sc1\n"
      "s_sub_u32 %1, %1, 1\n"
      "s_cbranch_scc1 2f\n"
      "s_sleep %5\n"
      "s_waitcnt vmcnt(0)\n"
      "v_cmp_ne_u64 vcc, %4, %0\n"
      "s_mov_b64 exec, vcc\n"
      "s_cbranch_vccnz 1b\n"
      "2:\n"
      "s_mov_b64 exec, -1\n"
      "s_waitcnt vmcnt(0)\n"
      : "=&v"(f), "+s"(cnt)
      : "v"(off), "s"(mkd(p.ws, WS_END)), "s"(tag), "n"(NAP)
      : "memory", "vcc", "scc");
}
__device__ __forceinline__ void set_flag(const Params& p, int off, u64 tag) {
  __builtin_amdgcn_raw_buffer_store_b64(v2u{(unsigned)tag, (unsigned)(tag >> 32)}, mk(p.ws, WS_END), off, 0, SC1);
}
__device__ __forceinline__ void lds_post(LAS char* L, int idx, int val) {
  asm volatile("" ::: "memory");
  *(volatile LAS int*)(L + L_FLAG + idx * 4) = val;
}
__device__ __forceinline__ void lds_post2(LAS char* L, int idx) {
  asm volatile("" ::: "memory");
  *(volatile LAS v2u*)(L + L_FLAG + idx * 4) = v2u{1u, 1u};
}
template <int NAP = 0, int OFF = 0>
__device__ __forceinline__ void lds_wait_at(int a, int val) {
  int cnt = 1 << 22, f;
  asm volatile(
      "1:\n"
      "ds_read_b32 %0, %2 offset:%5\n"
      "s_sub_u32 %1, %1, 1\n"
      "s_cbranch_scc1 2f\n"
      ".if %4\n s_sleep %4\n .endif\n"
      "s_waitcnt lgkmcnt(0)\n"
      "v_cmp_gt_i32 vcc, %3, %0\n"
      "s_cbranch_vccnz 1b\n"
      "2:\n"
      "s_waitcnt lgkmcnt(0)\n"
      : "=&v"(f), "+s"(cnt)
      : "v"(a), "s"(val), "n"(NAP), "n"(OFF)
      : "memory", "vcc", "scc");
}
template <int NAP = 0>
__device__ __forceinline__ void lds_wait(LAS char* L, int idx, int val) { lds_wait_at<NAP>((int)(size_t)(L + L_FLAG + idx * 4), val); }
template <int NAP = 0>
__device__ __forceinline__ void lds_wait4(LAS char* L, int idx, int lane) { lds_wait<NAP>(L, idx + (lane & 3), 1); }
__device__ __forceinline__ u64 tag_of(u64 e) { return e * 6364136223846793005ull + 1442695040888963407ull; }
__device__ __forceinline__ u64 ask_epoch(const Params& p) {
  u64 e;
  asm volatile("s_load_dwordx2 %0, %1, 0x0" : "=s"(e) : "s"(p.ws + WS_EPOCH) : "memory");
  return e;
}
__device__ __forceinline__ u64 get_tag(const Params& p) {
  u64 e;
  asm volatile("s_load_dwordx2 %0, %1, 0x0\n\ts_waitcnt lgkmcnt(0)" : "=s"(e) : "s"(p.ws + WS_EPOCH) : "memory");
  return tag_of(e);
}
__device__ __forceinline__ void touch(const char* a, LAS char* dump) {
  asm volatile("s_mov_b32 m0, %0\n\ts_nop 0\n\tglobal_load_lds_dword %1, off" ::"s"((unsigned)(size_t)dump), "v"(a) : "memory");
}
__device__ __forceinline__ void code_prefetch(u64 pc, LAS char* dump, int r, int lane) {
  dma16(mkd((const void*)pc, CODE_BYTES), dump, r * 1024 + lane * 16, 0);
}

constexpr int KV_IN = 18432;
struct KvPre {
  v4u m, sf;
  int sel;
  LAS char *ka[2], *va, *sa, *ta[2];
};
__device__ __forceinline__ void kv_pre(KvPre& k, LAS char* Li, LAS char* Lt, int cb, int lane) {
  const int c = lane & 15, g = lane >> 4, tok = 4 * (c & 3) + (c >> 2);
#pragma unroll
  for (int d = 0; d < 4; ++d) k.m[d] = (g & 1) == (c >> 3) && d == ((c >> 1) & 3) ? (c & 1 ? 0xffff0000u : 0xffffu) : 0u;
  k.sf = k.m & ((g >> 1) == (cb & 1) ? 0x3f803f80u : 0u);
  k.sel = g & 2 ? 0x03020302 : 0x01000100;
#pragma unroll
  for (int y = 0; y < 2; ++y) {
    const int row = 8 + tok - 2 * y - (g >> 1);
    k.ka[y] = Li + row * 128 + (((2 * cb + (g & 1)) ^ ((row >> 1) & 6)) << 4);
  }
  k.va = Li + 9216 + tok * 128 + (((4 * (cb >> 1) + g) ^ 4 ^ ((tok >> 1) & 6)) << 4);
  k.sa = Li + 17408 + cb * 128 + c * 8;
  k.ta[0] = Lt + (16 * cb + c) * 128 + (((2 * g) ^ (c & 7)) << 4);
  k.ta[1] = Lt + (16 * cb + c) * 128 + (((2 * g + 1) ^ (c & 7)) << 4);
  asm volatile("" : "+v"(k.m), "+v"(k.sf), "+v"(k.sel), "+v"(k.ka[0]), "+v"(k.ka[1]), "+v"(k.va), "+v"(k.sa), "+v"(k.ta[0]), "+v"(k.ta[1]));
}
template <int N>
__device__ __forceinline__ void kv_make(const KvPre& k) {
  v2u sk[N];
  v4u kf[N][4][2], vf[N][4];
#pragma unroll
  for (int n = 0; n < N; ++n) {
    sk[n] = *(LAS v2u*)(k.sa + n * KV_IN);
#pragma unroll
    for (int q = 0; q < 4; ++q) {
      vf[n][q] = *(LAS v4u*)(k.va + n * KV_IN + q * 2048);
#pragma unroll
      for (int y = 0; y < 2; ++y) kf[n][q][y] = *(LAS v4u*)(k.ka[y] + n * KV_IN + q * 2048);
    }
  }
  SB();
#pragma unroll
  for (int n = 0; n < N; ++n) {
    const v4u w0 = k.m & __builtin_amdgcn_perm(sk[n].x, sk[n].x, k.sel), w1 = k.m & __builtin_amdgcn_perm(sk[n].y, sk[n].y, k.sel);
    const f32x4 Z = {0.f, 0.f, 0.f, 0.f};
    v2u o2[4];
#pragma unroll
    for (int q = 0; q < 4; ++q) {
      const f32x4 o = mfma(kf[n][q][1], w1, mfma(kf[n][q][0], w0, Z)) * mfma(vf[n][q], k.sf, Z);
      o2[q] = v2u{pk(o[0], o[1]), pk(o[2], o[3])};
    }
    *(LAS v4u*)(k.ta[0] + n * 8192) = cat(o2[0], o2[1]);
    *(LAS v4u*)(k.ta[1] + n * 8192) = cat(o2[2], o2[3]);
  }
}
template <int N>
__device__ __forceinline__ void kv_store(const Params& p, LAS char* Lt, int n0, int pp, int G, int lane) {
  const int row = lane >> 3;
#pragma unroll
  for (int n = 0; n < N; ++n)
    st128<SC1>(*(LAS v4u*)(Lt + (n0 + n) * 1024 + lane * 16), mk(p.ws, WS_END),
               WS_KVT + (64 * G + 8 * (n0 + n) + row) * 4096 + 128 * pp + (((lane & 7) ^ row) << 4));
}

__device__ __forceinline__ int g2_step(int w, int rot, int k) {
  return k == 13 ? 26 + 27 * (w >> 1) : 27 * (w >> 1) + 13 * (w & 1) + (k + rot >= 13 ? k + rot - 13 : k + rot);
}
__device__ __forceinline__ int w2_block(int k) { return (0x63052417u >> (4 * (k & 7))) & 7; }
__device__ __forceinline__ void w2_src(int (&so)[4], int lane) {
  const int row = lane >> 3;
#pragma unroll
  for (int h = 0; h < 4; ++h) so[h] = (8 * h + row) * 1280 - 1024 * h + (((lane & 7) ^ (((row >> 1) & 3) << 1)) << 4);
}
__device__ __forceinline__ void w2_fetch(v4u rw2, LAS char* L, const int (&so)[4], int w, int rot, int k, int G2) {
  dma4(rw2, L + w2_block(k) * 16384 + w * 4096, so, g2_step(w, rot, k) * (32 * 1280) + G2 * 128);
}

__device__ __forceinline__ void stage_pads(LAS char* Lc, int lane) {
  if (lane < 16) *(LAS unsigned*)(Lc + (lane >> 3) * C_A2 + (lane & 7) * 4) = 0u;
  *(LAS v2u*)(Lc + C_B + (lane >> 4) * C_BS + (lane & 15) * 8) = v2u{0u, 0u};
}
template <int DELAYED>
__device__ __forceinline__ void stage_h(const Params& p, LAS char* Lc, int o) {
  asm volatile(
      "s_mov_b32 m0, %0\n\ts_mov_b64 exec, -2\n\tbuffer_load_dwordx4 %1, %2, 0 offen lds\n\ts_mov_b64 exec, -1\n\t"
      "buffer_load_dwordx4 %1, %2, 0 offen offset:1024 lds\n\tbuffer_load_dwordx4 %1, %2, 0 offen offset:2048 lds\n\t"
      "buffer_load_dwordx4 %1, %2, 0 offen offset:3072 lds" ::"s"((unsigned)(size_t)(Lc + DELAYED * C_A2 + 32)),
      "v"(o), "s"(mkd(p.hk - 4 * DELAYED, D * S * 2 + 4 * DELAYED))
      : "memory");
}
__device__ __forceinline__ int u_src(int ch, int lane) { return WS_KVT + ch * 4096 + (lane >> 3) * 512 + (lane & 7) * 16; }
__device__ __forceinline__ void stage_u(const Params& p, LAS char* Lc, int o) {
  asm volatile(
      "s_mov_b32 m0, %0\n s_nop 0\n"
      ".set e, 0\n .rept 4\n buffer_load_dwordx4 %1, %2, 0 offen offset:e*128 sc1 lds\n"
      " .if e < 3\n s_add_u32 m0, m0, %3\n s_nop 0\n .endif\n .set e, e+1\n .endr\n" ::"s"((unsigned)(size_t)(Lc + C_B + 128)),
      "v"(o), "s"(mkd(p.ws, WS_END)), "n"(C_BS - 128)
      : "memory", "scc");
}
__device__ __forceinline__ void stage_head(LAS char* Lc, v4u h, unsigned dbits) {
  const unsigned h01 = pk(blo(h.x) + __uint_as_float(dbits), bhi(h.x));
  *(LAS v4u*)(Lc + 32) = v4u{h01, h.y, h.z, h.w};
  *(LAS v4u*)(Lc + C_A2 + 32) = v4u{0u, h01, h.y, h.z};
}
#define OUT_AUX 0
#define FK_AT 2
#define H_PRIO 0
#define L_NB 3
#define ROT 0
#define E_YST 3
#define S_SPLIT 1
#define ORD 2
#define LATE_F 0
#define STR_(x) #x
#define STR(x) STR_(x)
#define TCH 1
#define RI 1
#define RJ 1
__device__ __forceinline__ int rot_of(int i, int j) { return (RI * i + RJ * j) % 10; }
__device__ __forceinline__ int kv_head(v4u kv, int who, int so, int ds, int dst, int dm, int v, int lane) {
  const int o = (lane >> 3) * 1280 + (((lane << 4) & 0x70) ^ (lane & 32));
  asm volatile(
      "s_cmp_eq_u32 %9, 4\n s_cbranch_scc0 1f\n s_setprio 3\n"
      "s_mov_b32 m0, %6\n s_cmp_eq_u32 %7, 0\n s_waitcnt lgkmcnt(0)\n"
      "s_cselect_b64 s[4:5], s[4:5], s[6:7]\n s_mov_b32 s6, %8\n s_mov_b32 s7, 0x20000\n s_and_b32 s5, s5, 0xffff\n"
      ".set h, 0\n .rept 2\n"
      " buffer_load_dwordx4 %2, s[4:7], %1 offen lds\n buffer_load_dwordx4 %3, s[4:7], %1 offen offset:1024 lds\n"
      " buffer_load_dwordx4 %4, s[4:7], %1 offen offset:2048 lds\n buffer_load_dwordx4 %5, s[4:7], %1 offen offset:3072 lds\n"
      " .if h == 0\n s_add_u32 m0, m0, %10\n s_add_u32 %1, %1, %11\n .endif\n .set h, 1\n"
      ".endr\n"
      "1:\n"
      : "+{s[4:7]}"(kv), "+&s"(so)
      : "v"(o ^ 64), "v"(o + 9216), "v"((o ^ 64) + 2 * 9216), "v"(o + 3 * 9216), "s"(dst), "s"(v), "n"(S * D * 2), "s"(who), "s"(dm), "s"(ds)
      : "memory", "scc");
  return o + so - 40 * 1280;
}
__device__ __forceinline__ void kv_rest(const Params& p, LAS char* L, int o, int jb, int lw, int lane) {
  int a = (jb >> 5) * 512 + lane * 16, b = lane >= 40 ? o : OOB;
  asm("" : "+v"(a), "+v"(b));
  dma16(mkd(lw ? p.sk : p.k, lw ? D * 8 : S * D * 2), L + L_KV + lw * 17408, lw ? a : b, 0);
}
struct LoadPre {
  int xo[4], wo[4], kst;
};
__device__ __forceinline__ void gemm1_first(const Params& p, LAS char* L, LoadPre& q, int ko, int i, int j, int jb, int lw, int lane) {
  SB();
  const int i16 = lane & 15, g = lane >> 4;
#pragma unroll
  for (int e = 0; e < 4; ++e) q.xo[e] = (128 * i + 64 * lw + 8 * e + (lane >> 3)) * 1280 - 1024 * e + (((lane & 7) ^ (4 * (e & 1) + g)) << 4);
#define KST i + (j >> 1)
  const int ij = KST;
  q.kst = ij - 10 * ((ij * 205) >> 11);
  TS(0);
#pragma unroll
  for (int h = 0; h < 2; ++h) dma4(mkd(p.x, S * D * 2), L + lw * 8192 + h * 4096, q.xo, q.kst * 128 + h * 32 * 1280);
  SB();
#pragma unroll
  for (int e = 0; e < 4; ++e) q.wo[e] = (32 * lw + 4 * e + g) * 3456 - 1024 * e + 256 * j + ((i16 ^ ((g | ((e >> 1) << 2)) << 1)) << 4);
#pragma unroll
  for (int h = 0; h < 2; ++h) dma4(mkd(p.w1, D * F * 2), L + L_W1 + lw * 8192 + h * 4096, q.wo, q.kst * (64 * 3456) + h * 16 * 3456);
}
__device__ __forceinline__ unsigned gemm1_load(const Params& p, LAS char* L, const LoadPre& q, v4u rw2, const int (&so)[4], int t0, int G2, int rot, int jb, int lw,
                                               int lane) {
  int ho = (2 * jb + lw) * 4096 + lane * 16;
  int xs = (16 * t0 + 48 * lw + (lane >> 3)) * 1280 + G2 * 128 + (((lane & 7) ^ (lane >> 3)) << 4);
  asm volatile("" : "+v"(ho), "+v"(xs));
  const v4u rx = mkd(p.x, S * D * 2), rw = mkd(p.w1, D * F * 2);
  {
    const int k1 = q.kst == 9 ? 0 : q.kst + 1, mb = (int)(size_t)L + lw * 8192;
    int sx = k1 * 128, sw = k1 * (64 * 3456), sm = mb + 16384, cnt = 9, t0, t1, t2;
#define LD4(a, b, c, d, r, s)                                                                                            \
  "buffer_load_dwordx4 %" #a ", %" #r ", %" #s " offen lds\n buffer_load_dwordx4 %" #b ", %" #r ", %" #s " offen offset:1024 lds\n" \
  "buffer_load_dwordx4 %" #c ", %" #r ", %" #s " offen offset:2048 lds\n buffer_load_dwordx4 %" #d ", %" #r ", %" #s " offen offset:3072 lds\n"
    asm volatile(
        "s_branch 2f\n"
        "1:\n" LD4(8, 9, 10, 11, 16, 4)
        "s_add_u32 m0, %6, 49152\n"
        "s_nop 0\n" LD4(12, 13, 14, 15, 17, 5)
        "2:\n"
        "s_mov_b32 m0, %2\n"
        "s_sub_u32 %3, %3, 1\n"
        "s_waitcnt vmcnt(16)\n"
        "s_barrier\n"
        "s_cbranch_scc1 3f\n"
        "4:\n" LD4(8, 9, 10, 11, 16, 0)
        "s_add_u32 m0, %2, 49152\n"
        "s_add_u32 %4, %0, 32*1280\n" LD4(12, 13, 14, 15, 17, 1)
        "s_add_u32 %6, %2, 4096\n"
        "s_mov_b32 m0, %6\n"
        "s_add_u32 %5, %1, 16*3456\n"
        "s_add_u32 %0, %0, 128\n"
        "s_add_u32 %1, %1, 64*3456\n"
        "s_cmp_eq_u32 %0, 1280\n"
        "s_cselect_b32 %0, 0, %0\n"
        "s_cselect_b32 %1, 0, %1\n"
        "s_add_u32 %2, %2, 16384\n"
        "s_cmp_eq_u32 %2, %7\n"
        "s_cselect_b32 %2, %18, %2\n"
        "s_branch 1b\n"
        "3:\n"
        : "+&s"(sx), "+&s"(sw), "+&s"(sm), "+&s"(cnt), "=&s"(t0), "=&s"(t1), "=&s"(t2)
        : "s"(mb + 49152), "v"(q.xo[0]), "v"(q.xo[1]), "v"(q.xo[2]), "v"(q.xo[3]), "v"(q.wo[0]), "v"(q.wo[1]), "v"(q.wo[2]), "v"(q.wo[3]), "s"(rx),
          "s"(rw), "s"(mb)
        : "memory", "scc"
        );
  }
  TS(4);
  v2u tg = *(volatile LAS v2u*)(L + L_TAG);
  asm volatile("s_waitcnt vmcnt(0)\n s_barrier" : "+v"(tg)::"memory");
  stage_h<1>(p, L + L_CONV + lw * CONV_BUF, ho);
#pragma unroll
  for (int h = 0; h < 6; ++h) dma16(rx, L + L_XS + (6 * lw + h) * 1024, xs, h * 8 * 1280);
  TS(5);
#pragma unroll
  for (int k = 0; k < 5; ++k) {
    if (k == L_NB) {
      TS(2);
      asm volatile("s_waitcnt vmcnt(%0)\n s_barrier" ::"n"(8 * L_NB) : "memory");
    }
    w2_fetch(rw2, L, so, 2 * lw, rot, k, G2);
    w2_fetch(rw2, L, so, 2 * lw + 1, rot, k, G2);
  }
  TS(1);
  return __builtin_amdgcn_readfirstlane(tg.y);
}

__device__ __forceinline__ unsigned gemm1_side(const Params& p, LAS char* L, int i, int j, int t0, int jb, int sw, int lane) {
  LAS char* Lc = L + L_CONV + sw * CONV_BUF;
  const int ch = 2 * jb + sw;
  if (sw) *(LAS int*)(L + L_FLAG + lane * 4) = 0;
  stage_pads(Lc, lane);
  u64 ep;
  unsigned db;
  v4u hh;
  asm volatile("s_load_dwordx2 %0, %3, 0x0\n s_load_dword %1, %4, 0x0\n s_load_dwordx4 %2, %5, 0x0"
               : "=&s"(ep), "=&s"(db), "=&s"(hh)
               : "s"(p.ws + WS_EPOCH), "s"(p.dv + jb * 4), "s"(p.hk + ch * 4096)
               : "memory");
  int g = lane >> 4, q = (lane >> 2) & 3;
  const char* tch = p.w2 + jb * 8192 + lane * 128;
  int o0 = WS_STAT + (128 * i + 64 * sw + lane) * 8, o1 = WS_CS + (128 * j + 64 * sw + lane) * 8;
  int o2 = WS_STAT + (16 * t0 + 48 * sw + min(lane, 47)) * 8, o3 = sw ? OOB : flag_at(FK + (ch >> 4) * 32 + (lane & 31));
  int ho = sw ? jb * 8192 + lane * 16 : u_src(ch, lane), ho2 = ho + 4096;
  LAS char *d0 = L + L_STAT + (64 * sw + lane) * 8, *d1 = L + L_CS + (64 * sw + lane) * 4;
  LAS char* d2 = L + L_STAT2 + (48 * sw + min(lane, 47)) * 8;
  int of = lane < 2 ? flag_at(FK + (4 * (jb >> 5) + 2 * sw + lane) * 32 + (jb & 31)) : OOB;
  int oc = i < 10 && !(i & 1) ? o1 + (i >> 1) * 14336 : OOB;
  asm volatile("" : "+v"(tch), "+v"(o0), "+v"(o1), "+v"(o2), "+v"(o3), "+v"(ho), "+v"(ho2), "+v"(d0), "+v"(d1), "+v"(d2), "+v"(of), "+v"(oc));
  asm volatile("s_barrier\n s_waitcnt lgkmcnt(0)" : "+s"(ep), "+s"(db), "+s"(hh)::"memory");
  lane = lane_id();
  g = lane >> 4, q = (lane >> 2) & 3;
  int ko[4], ca[4], ka = (int)(size_t)L + L_KT + sw * 4096 + lane * 16;
  v4u one[4];
#pragma unroll
  for (int u = 0; u < 4; ++u) {
    ko[u] = WS_KVT + (64 * (jb >> 5) + 32 * sw + 8 * u + (lane >> 3)) * 4096 + 128 * (jb & 31) + (((lane & 7) ^ (lane >> 3)) << 4);
    ca[u] = (int)(size_t)L + L_W1 + (8 * g + q) * 256 + (((4 * sw + u) ^ (q | ((g & 1) << 2))) << 5) + 8 * (lane & 3);
    const unsigned o = q == u ? 0x3f803f80u : 0u;
    one[u] = v4u{o, o, o, o};
    asm volatile("" : "+v"(ko[u]), "+v"(ca[u]), "+v"(one[u]));
  }
  asm volatile("" : "+v"(ka));
  stage_head(Lc, hh, sw ? db & 0xffff0000u : db << 16);
  const u64 tag = tag_of(ep);
  const unsigned t32 = (unsigned)(tag >> 32);
  *(LAS v2u*)(L + L_TAG) = v2u{(unsigned)tag, t32};
  const v4u rwd = mkd(p.ws, WS_END);
  TS(1);
  BAR();
  {
    v4u sa[2][4][2], kt[4];
    auto rd = [&](int s) {
#pragma unroll
      for (int u = 0; u < 4; ++u)
#pragma unroll
        for (int ks = 0; ks < 2; ++ks) {
          LAS char* aa = (LAS char*)(size_t)ca[u] + s * 16384 + ks * 8192;
          sa[s][u][ks] = cat(trread(aa), trread(aa + 1024));
        }
      SB();
    };
    f32x4 col = {0.f, 0.f, 0.f, 0.f};
    auto mm = [&](int s) {
#pragma unroll
      for (int u = 0; u < 4; ++u)
#pragma unroll
        for (int ks = 0; ks < 2; ++ks) col = mfma(one[u], sa[s][u][ks], col);
      SB();
    };
#pragma unroll
    for (int u = 0; u < 4; ++u) kt[u] = *(LAS v4u*)(size_t)(ka + u * 1024);
    rd(0);
    BAR();
    SB();
    rd(1);
#pragma unroll
    for (int u = 0; u < 4; ++u) st128<SC1>(kt[u], mk(p.ws, WS_END), ko[u]);
    SB();
    BAR();
    SB();
    mm(0);
    mm(1);
    asm volatile("s_barrier\n"
                 "s_waitcnt vmcnt(0)" : "+v"(col)::"memory");
    set_flag(p, of, tag);
    __builtin_amdgcn_raw_buffer_store_b64(v2u{__float_as_uint(col[0]), t32}, mk(p.ws, WS_END), oc, 0, SC1);
    SB();
  }
  TS(2);
  v2u g0, g2, g1[5];
  u64 fk;
#define S_FETCH                                                                                                        \
  "buffer_load_dwordx2 %0, %10, %18, 0 offen sc1\n buffer_load_dwordx2 %1, %11, %18, 0 offen sc1\n"                    \
  "buffer_load_dwordx2 %2, %12, %18, 0 offen sc1\n buffer_load_dwordx2 %3, %13, %18, 0 offen sc1\n"                    \
  "buffer_load_dwordx2 %4, %14, %18, 0 offen sc1\n buffer_load_dwordx2 %5, %15, %18, 0 offen sc1\n"                    \
  "buffer_load_dwordx2 %6, %16, %18, 0 offen sc1\n buffer_load_dwordx2 %7, %17, %18, 0 offen sc1\n"
#define S_OUT "=&v"(g0), "=&v"(g1[0]), "=&v"(g1[1]), "=&v"(g1[2]), "=&v"(g1[3]), "=&v"(g1[4]), "=&v"(g2), "=&v"(fk)
#define S_IN "v"(o0), "v"(o1), "v"(o1 + 14336), "v"(o1 + 2 * 14336), "v"(o1 + 3 * 14336), "v"(o1 + 4 * 14336), "v"(o2), "v"(o3), "s"(rwd)
  asm volatile(
      "s_barrier\n"
      "s_barrier\n s_barrier\n s_barrier\n"
      S_FETCH
      "s_cmp_eq_u32 %8, 0\n"
      "s_mov_b32 m0, %9\n"
      "s_barrier\n"
      "s_cbranch_scc1 1f\n"
      ".set c, 0\n .rept 2\n"
      " s_mov_b64 exec, -2\n buffer_load_dwordx4 %19, %21, 0 offen lds\n s_mov_b64 exec, -1\n"
      " buffer_load_dwordx4 %19, %21, 0 offen offset:1024 lds\n buffer_load_dwordx4 %19, %21, 0 offen offset:2048 lds\n"
      " buffer_load_dwordx4 %19, %21, 0 offen offset:3072 lds\n"
      " s_add_u32 m0, m0, %24+c*(%25-%24)\n v_mov_b32 %19, %20\n"
      " .set c, c+1\n"
      ".endr\n"
      "global_load_lds_dword %22, off\n"
      "s_waitcnt vmcnt(9)\n"
      "v_mov_b64 %7, %23\n"
      "s_branch 2f\n"
      "1:\n"
      "s_waitcnt vmcnt(0)\n"
      "v_cmp_ne_u64 vcc, %23, %7\n"
      "s_cbranch_vccnz 2f\n"
      ".set e, 0\n .rept 8\n"
      " .if e < 4\n buffer_load_dwordx4 %19, %18, 0 offen offset:e*128 sc1 lds\n .else\n buffer_load_dwordx4 %20, %18, 0 offen offset:(e-4)*128 sc1 lds\n .endif\n"
      " .if e == 3\n s_add_u32 m0, m0, %24-3*%26\n s_nop 0\n .elseif e < 7\n s_add_u32 m0, m0, %26\n s_nop 0\n .endif\n"
      " .set e, e+1\n"
      ".endr\n"
      "2:\n"
      : S_OUT
      : "s"(sw), "s"((unsigned)(size_t)(L + L_CONV + (sw ? 32 : C_B + 128))), S_IN, "v"(ho), "v"(ho2), "s"(mkd(p.hk, D * S * 2)), "v"(tch), "s"(tag),
        "n"(CONV_BUF), "n"(L_DUMP - L_CONV - CONV_BUF - 32), "n"(C_BS - 128)
      : "memory", "scc", "vcc");
  const bool kv_there = __builtin_amdgcn_ballot_w64(fk != tag) == 0;
  TS(3);
  auto stale = [&]() {
    unsigned a = g0.y & g2.y, o = g0.y | g2.y;
#pragma unroll
    for (int n = 0; n < 5; ++n) {
      a &= g1[n].y;
      o |= g1[n].y;
    }
    return __builtin_amdgcn_ballot_w64(a != t32 || o != t32) != 0;
  };
  f32x2 st0, st2;
  float cs;
  auto digest = [&]() {
    cs = __uint_as_float(g1[0].x);
#pragma unroll
    for (int n = 1; n < 5; ++n) cs += __uint_as_float(g1[n].x);
    st0 = f32x2{blo(g0.x), __builtin_amdgcn_rcpf(bhi(g0.x))};
    st2 = f32x2{blo(g2.x), __builtin_amdgcn_rcpf(bhi(g2.x))};
  };
  const bool late = stale();
  digest();
  asm volatile("s_barrier" : "+v"(st0), "+v"(st2), "+v"(cs)::"memory");
  if (__builtin_expect(late, 0)) {
    int spin = SPIN_CAP;
#pragma unroll 1
    do asm volatile(S_FETCH "s_waitcnt vmcnt(0)" : S_OUT : "n"(0), "n"(0), S_IN : "memory");
    while (stale() && --spin);
    digest();
  }
  *(LAS f32x2*)d0 = st0;
  *(LAS float*)d1 = cs;
  lds_post2(L, F_STAT + 2 * sw);
  *(LAS f32x2*)d2 = st2;
  TS(5);
  RAW(14, __builtin_amdgcn_readfirstlane(kv_there + 1000 * late));
  if (__builtin_expect(!kv_there, 0)) {
    wait_flag<2>(p, o3, tag);
    stage_u(p, L + L_CONV, ho);
    stage_u(p, L + L_CONV + CONV_BUF, ho2);
  }
  asm volatile("s_waitcnt vmcnt(0) lgkmcnt(0)" ::: "memory");
  TS(6);
  asm volatile("s_barrier" ::: "memory");
  TS(4);
  return t32;
}

struct ConvPre {
  v4u ba;
  int aa, fl, lb, cw, kw, yr, yo;
};
__device__ __forceinline__ void conv_pre(ConvPre& c, LAS char* L, LAS char* Lc, int ch, int me, int lane) {
  const int pw = me & 1, col = lane & 15, g = lane >> 4, b = (int)(size_t)Lc;
  c.aa = b + pw * C_A2 + 32 + 64 * g - 8 * ((lane >> 2) & 3) + 8 * (lane & 3);
#pragma unroll
  for (int e = 0; e < 4; ++e) {
    const int u = 7 + col - 2 * g + 8 * (e >> 1) + (e & 1);
    c.ba[e] = b + C_B + pw * 64 + ((u >> 2) & 3) * C_BS + (u >> 4) * 128 + (u & 3) * 8;
  }
  c.fl = (int)(size_t)L + L_FLAG + (F_PAIR + me) * 4;
  c.lb = b + lane * 16;
  const int Q = (4 * col + g + 1) & 63;
  c.cw = b + C_B + pw * 2304 + (Q & 3) * 256 + (Q >> 2) * 16;
  c.kw = b + 2048 + pw * 2048 + 32 * col + 8 * g;
  c.yr = b + 2048 + (11 * pw + g) * 192 + 8 * col;
  c.yo = WS_YT + ch * YS + pw * 11 * 256 + lane * 16;
  asm volatile("" : "+v"(c.ba), "+v"(c.aa), "+v"(c.fl), "+v"(c.lb), "+v"(c.cw), "+v"(c.kw), "+v"(c.yr), "+v"(c.yo));
}
template <typename T>
__device__ __forceinline__ LAS T& at(int a) { return *(LAS T*)(size_t)a; }
template <int H>
__device__ __forceinline__ void conv_send(const ConvPre& cp, const f32x32& z0) {
#pragma unroll
  for (int c = 4 * H; c < 4 * H + 4; ++c) at<f32x4>(cp.lb + c * 1024) = f32x4{z0[4 * c], z0[4 * c + 1], z0[4 * c + 2], z0[4 * c + 3]};
  asm volatile("; half %0" ::"n"(H) : "memory");
}
template <int H>
__device__ __forceinline__ void conv_keep(const ConvPre& cp, const f32x32& z0) {
  f32x4 y[4];
  const f32x4 c0 = at<f32x4>(cp.lb + C_B + H * 1024), c1 = at<f32x4>(cp.lb + C_B + 2304 + H * 1024);
#pragma unroll
  for (int c = 0; c < 4; ++c) y[c] = at<f32x4>(cp.lb + (4 * H + c) * 1024);
  SB();
#pragma unroll
  for (int c = 0; c < 4; ++c) {
    const int b = 16 * H + 4 * c;
    at<v2u>(cp.kw + 512 * c) = v2u{pks(y[c][0] + z0[b] + c0[c] + c1[c], y[c][1] + z0[b + 1]), pks(y[c][2] + z0[b + 2], y[c][3] + z0[b + 3])};
  }
  asm volatile("; half %0" ::"n"(H) : "memory");
}
__device__ __forceinline__ void pair_sync(const ConvPre& cp, int val) {
  asm volatile("" ::: "memory");
  at<volatile int>(cp.fl) = val;
  lds_wait_at(cp.fl ^ 4, val);
}
__device__ __forceinline__ void conv(const Params& p, const ConvPre& cp, int me, int round, bool g1, u32x32& held, unsigned t32) {
  const int pw = me & 1;
  u32x32 a0, a1, b0, b1;
  asm volatile(
      ".set n, 0\n"
      ".rept 16\n"
      " ds_read_b64_tr_b16 v[4*n:4*n+1], %4 offset:256*n+32\n"
      " ds_read_b64_tr_b16 v[4*n+2:4*n+3], %4 offset:256*n\n"
      " ds_read_b64 v[64+4*n:65+4*n], v[192+2*(n&1)] offset:32+128*(n/2)\n"
      " ds_read_b64 v[66+4*n:67+4*n], v[193+2*(n&1)] offset:32+128*(n/2)\n"
      " .set n, n+1\n"
      ".endr\n"
      : "=&{v[0:31]}"(a0), "=&{v[32:63]}"(a1), "=&{v[64:95]}"(b0), "=&{v[96:127]}"(b1)
      : "v"(cp.aa), "{v[192:195]}"(cp.ba)
      : "memory");
  if (__builtin_expect(g1, 1)) {
    asm volatile("s_waitcnt vmcnt(8) lgkmcnt(0)" ::: "memory");
    ST(8);
    asm volatile("s_barrier" ::: "memory");
  } else if (me >= 4) {
    int a = cp.fl;
    asm volatile("" : "+v"(a));
    lds_wait_at<2>(a - 16, 2);
  }
  ST(9);
  f32x32 z1, z0;
  asm volatile(
      "s_waitcnt lgkmcnt(0)\n"
      "s_setprio 1\n"
      ".set m, 0\n"
      ".set r, 0\n"
      ".set y, 0\n"
      ".rept 2\n"
      " .set c, 7\n"
      " .rept 8\n"
      "  .set n, 0\n"
      "  .rept 2*c+2\n"
      "   .if m == 72\n"
      "    s_waitcnt lgkmcnt(4)\n"
      "   .endif\n"
      "   .if m == 84\n"
      "    s_waitcnt lgkmcnt(0)\n"
      "    ds_write_b32 %8, %9\n"
      "   .endif\n"
      "   .set z, 128+32*(m/72)+4*c\n"
      "   .set b, 64+4*(2*c+1-n)\n"
      "   .if (m < 72) && (n == 0)\n"
      "    v_mfma_f32_16x16x32_bf16 v[z:z+3], v[0:3], v[b:b+3], 0\n"
      "   .else\n"
      "    v_mfma_f32_16x16x32_bf16 v[z:z+3], v[4*n:4*n+3], v[b:b+3], v[z:z+3]\n"
      "   .endif\n"
      "   .set cr, 7-r/4\n"
      "   .set cy, 7-y/4\n"
      "   .if (r < 32) && ((m >= 72) || (c < cr) || ((c == cr) && (n >= 2)))\n"
      "    .set b, 2*cr+1-(r&3)/2\n"
      "    ds_read_b64 v[64+4*b+2*(r&1):65+4*b+2*(r&1)], v[192+2*(b&1)+(r&1)] offset:128*(b/2)\n"
      "    .set r, r+1\n"
      "   .elseif (y < 32) && (m >= 80-cy*(cy+1))\n"
      "    .if y&3\n"
      "     v_mov_b32 v[160+4*cy+(y&3)], v[127+4*cy+(y&3)]\n"
      "    .else\n"
      "     v_mov_b32 v[160+4*cy], 0\n"
      "    .endif\n"
      "    .set y, y+1\n"
      "   .endif\n"
      "   .set m, m+1\n"
      "   .set n, n+1\n"
      "  .endr\n"
      "  .set c, c-1\n"
      " .endr\n"
      ".endr\n"
      "s_setprio 3\n"
      "s_waitcnt vmcnt(0)\n"
      "s_nop 5\n"
      : "=&{v[128:159]}"(z1), "=&{v[160:191]}"(z0), "+{v[64:95]}"(b0), "+{v[96:127]}"(b1), "+{v[224:255]}"(held)
      : "{v[0:31]}"(a0), "{v[32:63]}"(a1), "{v[192:195]}"(cp.ba), "v"(cp.fl), "v"(2 + 8 * round)
      : "memory");
  ST(10);
  const int lane = lane_id();
  lds_wait_at(cp.fl ^ 4, 2 + 8 * round);
  {
    const bool last = lane == 63;
    at<f32x4>(cp.cw) = f32x4{last ? 0.f : z1[3], last ? z1[3] : z1[7], last ? z1[7] : z1[11], last ? z1[11] : z1[15]};
    at<f32x4>(cp.cw + 1024) = f32x4{last ? z1[15] : z1[19], last ? z1[19] : z1[23], last ? z1[23] : z1[27], last ? z1[27] : z1[31]};
  }
  if (pw) conv_send<0>(cp, z0);
  else conv_send<1>(cp, z0);
  pair_sync(cp, 3 + 8 * round);
  if (pw) conv_keep<1>(cp, z0);
  else conv_keep<0>(cp, z0);
  int r2 = cp.yr - (lane & (32 * pw)) * 4, o2 = lane < 48 ? cp.yo + 2048 : OOB;
  asm volatile("" : "+v"(r2), "+v"(o2));
  pair_sync(cp, 4 + 8 * round);
  v2u ya[3], yb[3];
#pragma unroll
  for (int n = 0; n < 3; ++n) {
    LAS char* a = (LAS char*)(size_t)(n < 2 ? cp.yr : r2) + n * 768;
    ya[n] = trread(a);
    yb[n] = trread(a + 128);
  }
  SB();
#pragma unroll
  for (int n = 0; n < E_YST; ++n) st128<SC1>(v4u{ya[n].x, ya[n].y, yb[n].x, t32}, mk(p.ws, WS_END), n < 2 ? cp.yo + n * 1024 : o2);
  ST(12);
}

#define ASM_TS(n)
#define ASM_TC(n)
#define V10(a) "v" #a "0", "v" #a "1", "v" #a "2", "v" #a "3", "v" #a "4", "v" #a "5", "v" #a "6", "v" #a "7", "v" #a "8", "v" #a "9"
__device__ __forceinline__ int xs_at(int a, int lane) { return (lane & 15) * 128 + (((2 * a + (lane >> 5)) ^ (lane & 7)) << 4) + ((lane >> 4) & 1) * 8; }
#define F_NAP 1
#define F_PRIO 1
__device__ __forceinline__ void finish(const Params& p, LAS char* L, const v2u (&xv)[NT], int t0, int rng, int G2, int a, unsigned t32, int lane) {
  const int i16 = lane & 15, g = lane >> 4;
  f32x2 st[NT];
#pragma unroll
  for (int u = 0; u < NT; ++u) st[u] = *(LAS f32x2*)(L + L_STAT2 + (16 * u + i16) * 8);
  const v2u bv = *(LAS v2u*)(L + L_B2 + a * 32 + g * 8);
  SB();
  f32x32 x, s;
#pragma unroll
  for (int u = 0; u < NT; ++u) {
    x[4 * u] = blo(xv[u].x), x[4 * u + 1] = bhi(xv[u].x), x[4 * u + 2] = blo(xv[u].y), x[4 * u + 3] = bhi(xv[u].y);
#pragma unroll
    for (int r = 0; r < 4; ++r) {
      float d = x[4 * u + r] - st[u].x;
      asm("" : "+v"(d));
      d *= st[u].y;
      asm("" : "+v"(d));
      s[4 * u + r] = d;
    }
  }
  x[24] = blo(bv.x), x[25] = bhi(bv.x), x[26] = blo(bv.y), x[27] = bhi(bv.y);
  x[28] = x[30] = -1.442695041f, x[29] = x[31] = 1.f;
  const int zb = (int)(size_t)L + a * 1024, yb = WS_YT + (64 * G2 + 16 * a) * YS + rng * 256;
  s[24] = __int_as_float(zb + lane * 16), s[25] = s[26] = __int_as_float(zb + 65536 + lane * 16);
  s[27] = __int_as_float((int)(size_t)L + L_FLAG + (F_DONE + (lane & 3)) * 4);
  s[28] = __int_as_float(4 * g * YS + i16 * 16);
  s[29] = s[30] = s[31] = __int_as_float((16 * t0 + i16) * 1280 + G2 * 128 + a * 32 + g * 8);
  ST(13);
  int cnt, tmp;
  asm volatile(
      ".macro OP8 op, d, a, b, da, db\n"
      " .set e, 0\n .rept 8\n \\op v[\\d+e], v[\\a+(e&3)+\\da*(e/4)], v[\\b+(e&3)+\\db*(e/4)]\n .set e, e+1\n .endr\n"
      ".endm\n"
      ".macro PK4 op, d, a, b, da, db\n"
      " .set e, 0\n .rept 4\n .set a, \\a+(e&2)+\\da*(e/4)\n .set b, \\b+(e&2)+\\db*(e/4)\n"
      "  \\op v[\\d+e:\\d+e+1], v[a:a+1], v[b:b+1]\n .set e, e+2\n .endr\n"
      ".endm\n"
      ".macro TR8 op\n"
      " .set e, 0\n .rept 8\n \\op v[120+e], v[120+e]\n .set e, e+1\n .endr\n"
      ".endm\n"
      ".macro YLD\n"
      " buffer_load_dwordx4 v[96:99], v60, %4, %6 offen sc1\n buffer_load_dwordx4 v[100:103], v60, %4, %7 offen sc1\n"
      " buffer_load_dwordx4 v[104:107], v60, %4, %8 offen sc1\n buffer_load_dwordx4 v[108:111], v60, %4, %9 offen sc1\n"
      ".endm\n"
      ".macro PUT u, o\n"
      " v_cvt_pk_bf16_f32 v[\\o], v[\\o], v[\\o+1]\n v_cvt_pk_bf16_f32 v[\\o+1], v[\\o+2], v[\\o+3]\n"
      " s_mov_b32 %3, \\u*16*1280\n"
      " buffer_store_dwordx2 v[\\o:\\o+1], v61, %5, %3 offen\n"
      ".endm\n"
      ".macro ZDT P\n"
      " .set n, 0\n"
      " .rept 8\n"
      "  .set t, 2*\\P+((n/4)^(n&1))\n"
      "  ds_read_b128 v[64+4*n:67+4*n], v[56+t/4] offset:(t&3)*16384+(n&3)*4096\n"
      "  .set n, n+1\n"
      " .endr\n"
      ".endm\n"
      ".macro ZRD P\n"
      " s_mov_b32 %2, 0x400000\n"
      " 8:\n"
      " ds_read_b32 v128, v59 offset:16*\\P\n"
      " s_sub_u32 %2, %2, 1\n"
      " s_cbranch_scc1 9f\n"
      " .if \\P == 0\n s_sleep 1\n .endif\n"
      " s_waitcnt lgkmcnt(0)\n"
      " v_cmp_eq_u32 vcc, 0, v128\n"
      " s_cbranch_vccnz 8b\n"
      " 9:\n"
      " ZDT \\P\n"
      ".endm\n"
      "s_branch 4f\n"
      "1:\n"
      "YLD\n"
      "s_branch 2f\n"
      "4:\n"
      ".set P, 0\n"
      ".rept 2\n"
      " ZRD P\n"
      " .if P == 0\n YLD\n" ASM_TS(0) " .endif\n"
      " s_waitcnt lgkmcnt(0)\n"
      " OP8 v_add_f32, 112, 64, 68, 16, 16\n"
      " OP8 v_add_f32, 120, 72, 76, 16, 16\n"
      " OP8 v_add_f32, 112, 112, 24, 4, 0\n"
      " OP8 v_add_f32, 112, 112, 120, 4, 4\n"
      " .set e, 0\n .rept 8\n v_mul_f32 v[120+e], v28, v[112+e]\n .set e, e+1\n .endr\n"
      " TR8 v_exp_f32\n"
      " OP8 v_mul_f32, 32+8*P, 32+8*P, 112, 4, 4\n"
      " .set e, 0\n .rept 8\n v_add_f32 v[120+e], 1.0, v[120+e]\n .set e, e+1\n .endr\n"
      " TR8 v_rcp_f32\n"
      " s_nop 0\n"
      " OP8 v_mul_f32, 32+8*P, 32+8*P, 120, 4, 4\n"
      " .set P, P+1\n"
      ".endr\n" ASM_TS(1)
      "s_mov_b32 %2, 0x8000\n"
      "2:\n"
      "s_waitcnt vmcnt(0)\n"
      "v_xor_b32 v112, %10, v99\n v_xor_b32 v113, %10, v103\n v_xor_b32 v114, %10, v107\n v_xor_b32 v115, %10, v111\n"
      "v_or3_b32 v112, v112, v113, v114\n v_or_b32 v112, v112, v115\n"
      "s_sub_u32 %2, %2, 1\n"
      "v_cmp_ne_u32 vcc, 0, v112\n"
      "s_cbranch_scc1 3f\n"
      "s_cbranch_vccnz 1b\n"
      "3:\n" ASM_TS(2)
      "ds_read_b32 v128, v59 offset:32\n"
      "ZDT 2\n"
      ".set u, 5\n"
      ".rept 6\n"
      " .set o, 112+4*(u&3)\n"
      " .set r, 0\n"
      " .rept 4\n"
      "  .if u&1\n v_and_b32 v[o+r], 0xffff0000, v[96+4*r+u/2]\n .else\n v_lshlrev_b32 v[o+r], 16, v[96+4*r+u/2]\n .endif\n"
      "  .set r, r+1\n"
      " .endr\n"
      " .set r, 0\n"
      " .rept 4\n"
      "  .if u < 4\n v_fma_f32 v[o+r], v[o+r], v[32+4*u+r], v[4*u+r]\n .else\n v_mul_f32 v[32+4*u+r], v[o+r], v[32+4*u+r]\n .endif\n"
      "  .set r, r+1\n"
      " .endr\n"
      " .if u < 4\n PUT u, o\n .endif\n"
      " .set u, u-1\n"
      ".endr\n"
      "s_waitcnt lgkmcnt(0)\n"
      "v_cmp_eq_u32 vcc, 0, v128\n"
      "s_cbranch_vccz 5f\n"
      "ZRD 2\n"
      "s_waitcnt lgkmcnt(0)\n"
      "5:\n"
      "PK4 v_pk_add_f32, 112, 64, 68, 16, 16\n"
      "PK4 v_pk_add_f32, 120, 72, 76, 16, 16\n"
      "PK4 v_pk_add_f32, 112, 112, 24, 4, 0\n"
      "PK4 v_pk_add_f32, 112, 112, 120, 4, 4\n"
      ".set e, 0\n .rept 4\n v_pk_mul_f32 v[120+e:121+e], v[112+e:113+e], v[28:29] op_sel_hi:[1,0]\n .set e, e+2\n .endr\n"
      "TR8 v_exp_f32\n"
      "PK4 v_pk_mul_f32, 48, 48, 112, 4, 4\n"
      ".set e, 0\n .rept 4\n v_pk_add_f32 v[120+e:121+e], v[120+e:121+e], v[28:29] op_sel:[0,1]\n .set e, e+2\n .endr\n"
      "TR8 v_rcp_f32\n"
      "s_nop 0\n"
      ".set e, 0\n .rept 4\n v_pk_fma_f32 v[112+e:113+e], v[120+e:121+e], v[48+e:49+e], v[16+e:17+e]\n .set e, e+2\n .endr\n"
      "PUT 4, 112\n"
      "PUT 5, 116\n"
      ".purgem OP8\n .purgem PK4\n .purgem TR8\n .purgem YLD\n .purgem ZDT\n .purgem PUT\n .purgem ZRD\n"
      : "+{v[0:31]}"(x), "+{v[32:63]}"(s), "=&s"(cnt), "=&s"(tmp)
      : "s"(mkd(p.ws, WS_END)), "s"(mkd(p.out, S * D * 2)), "s"(yb), "s"(yb + YS), "s"(yb + 2 * YS), "s"(yb + 3 * YS), "s"(t32)
      : "memory", "scc", "vcc", "v64", "v65", "v66", "v67", "v68", "v69", V10(7), V10(8), V10(9), V10(10), V10(11), V10(12), V10(13)
  );
  ST(7);
  RAW(5, __builtin_amdgcn_readfirstlane(__float_as_int(x[0])));
  RAW(6, __builtin_amdgcn_readfirstlane(__float_as_int(x[1])));
  RAW(11, __builtin_amdgcn_readfirstlane(__float_as_int(x[2])));
}

#define WRN "2"
#ifndef M_PRIO
#define M_PRIO "2"
#endif
#define WB "2"
#define B9 "2"
#define HB "7"
#define HN "31"
__device__ __forceinline__ void mma_waves(const Params& p, LAS char* L, int i, int j, bool job, int t0, int G2, int rot, int w, int lane) {
  const int fh = w & 1, th = w >> 1, i16 = lane & 15, g = lane >> 4, q = (lane >> 2) & 3, Lb = (int)(size_t)L;
  const int fb = 128 * j + 64 * fh;
  v2u b1v[4];
#pragma unroll
  for (int m = 0; m < 4; ++m) b1v[m] = __builtin_amdgcn_raw_buffer_load_b64(mk(p.b1, F * 2), (fb + 16 * m + 4 * g) * 2, 0, 0);
  const v2u b2v = __builtin_amdgcn_raw_buffer_load_b64(mk(p.b2, D * 2), (64 * G2 + 16 * w + 4 * g) * 2, 0, 0);
  SB();
  const int sb = 27 * th + 13 * fh, ss = 26 + 27 * th;
  i32x16 va, vb;
#pragma unroll
  for (int m = 0; m < 4; ++m) va[m] = Lb + L_W1 + (8 * g + q) * 256 + (((4 * fh + m) ^ (q | ((g & 1) << 2))) << 5) + 8 * (lane & 3);
#pragma unroll
  for (int ks = 0; ks < 2; ++ks) va[4 + ks] = Lb + (64 * th + i16) * 128 + (((4 * ks + g) ^ (i16 >> 1)) << 4);
  va[6] = Lb + L_STAT + (64 * th + i16) * 8;
  va[7] = Lb + L_CS + (64 * fh + 4 * g) * 4;
  va[8] = Lb + L_FLAG + w * 4;
  va[9] = Lb + L_FLAG + (lane & 3) * 4;
  va[10] = lane * 16 + WS_HT + (4 * j + 2 * fh) * HS + (8 * i + 4 * th) * 1024;
  va[11] = va[10] + HS;
  va[14] = 1;
  {
    int so[4];
    w2_src(so, lane);
    va[15] = so[0];
  }
  {
    const int rr = 4 * g + q;
#pragma unroll
    for (int dd = 0; dd < 4; ++dd) {
      vb[dd] = Lb + w * 4096 + rr * 128 + ((dd ^ ((rr >> 1) & 3)) << 5) + 8 * (lane & 3);
      vb[4 + dd] = vb[dd] + 65536;
    }
    vb[8] = lane * 16 + fh * 1024;
    vb[9] = lane * 16 + 1024 - fh * 1024;
    vb[10] = vb[8] + 4096;
    vb[11] = vb[9] + 4096;
    vb[12] = Lb + w * 4096 + lane * 16;
    vb[13] = vb[12] + 65536;
    vb[14] = vb[15] = va[8];
    va[12] = lane == 0 ? flag_at(FH + (2 * i + th) * 32 + 2 * j + fh) : OOB;
    const int tl = min(t0 + 4 * (lane >> 3), t0 + NT - 1), c = lane & 7;
    va[13] = flag_at(FH + (tl >> 2) * 32 + (c == 7 ? ss >> 1 : (sb >> 1) + c));
  }
  f32x32 acc[2];
  asm volatile(
      ".set n, 0\n"
      ".rept 32\n"
      " v_mov_b64 v[n:n+1], 0\n"
      " .set n, n+2\n"
      ".endr\n"
      : "={v[0:31]}"(acc[0]), "={v[32:63]}"(acc[1]), "+{v[128:143]}"(va));
  i32x16 sh;
  i32x8 sw;
#pragma unroll
  for (int k = 0; k < 16; ++k)
    sh[k] = WS_HT + t0 * 1024 + g2_step(w, rot, min(k, 13)) * HS;
#pragma unroll
  for (int k = 0; k < 8; ++k) sw[k] = G2 * 128 + g2_step(w, rot, 5 + k) * 40960;
  int role = __builtin_amdgcn_readfirstlane((fb < F ? 1 : 0) + (job ? 2 : 0));
  asm volatile("" : "+{s[52:67]}"(sh), "+{s[68:75]}"(sw), "+s"(role));
  KvPre kp;
  kv_pre(kp, L + L_KV, L + L_KT, w, lane);
  BAR();
  kv_make<1>(kp);
  SB();
  f32x16 bb;
#pragma unroll
  for (int m = 0; m < 4; ++m) {
    bb[4 * m] = blo(b1v[m].x);
    bb[4 * m + 1] = bhi(b1v[m].x);
    bb[4 * m + 2] = blo(b1v[m].y);
    bb[4 * m + 3] = bhi(b1v[m].y);
  }
  *(LAS v2u*)(L + L_B2 + w * 32 + g * 8) = b2v;
  {
    int cnt, zo = 65536 - Lb - w * 4096;
    asm volatile(
        ".macro G1RD n, s, sl, ks\n"
        " .if (\\n %% 3) == 2\n"
        "  ds_read_b128 v[\\s+16+4*(\\n/3):\\s+19+4*(\\n/3)], v[132+\\ks] offset:\\sl*16384+(\\n/3)*2048\n"
        " .else\n"
        "  ds_read_b64_tr_b16 v[\\s+4*(\\n/3)+2*(\\n%%3):\\s+4*(\\n/3)+2*(\\n%%3)+1], v[128+\\n/3] offset:\\sl*16384+\\ks*8192+(\\n%%3)*1024\n"
        " .endif\n"
        ".endm\n"
        ".macro STRD n\n"
        " .if \\n == 0\n"
        "  ds_read_b32 v164, v137 offset:%c12*4\n"
        " .elseif \\n < 5\n"
        "  ds_read_b64 v[166+2*\\n:167+2*\\n], v134 offset:(\\n-1)*128\n"
        " .else\n"
        "  ds_read_b128 v[156+4*\\n:159+4*\\n], v135 offset:(\\n-5)*64\n"
        " .endif\n"
        ".endm\n"
        ".macro G1H c, s, sl, ks, st\n"
        " .set n, 0\n"
        " .rept 16\n"
        "  v_mfma_f32_16x16x32_bf16 v[4*n:4*n+3], v[\\c+4*(n&3):\\c+4*(n&3)+3], v[\\c+16+4*(n/4):\\c+19+4*(n/4)], v[4*n:4*n+3]\n"
        "  .if (n < 12) && (\\sl >= 0)\n"
        "   G1RD n, \\s, \\sl, \\ks\n"
        "  .endif\n"
        "  .if (n >= 4) && (n < 13) && \\st\n"
        "   .set m, n-4\n"
        "   STRD m\n"
        "  .endif\n"
        "  .set n, n+1\n"
        " .endr\n"
        ".endm\n"
        ".macro LWAIT a, f, v\n"
        " s_mov_b32 %7, 0x400000\n"
        " 8:\n"
        " ds_read_b32 v255, \\a offset:\\f*4\n"
        " s_sub_u32 %7, %7, 1\n"
        " s_cbranch_scc1 9f\n"
        " s_waitcnt lgkmcnt(0)\n"
        " v_cmp_gt_i32 vcc, \\v, v255\n"
        " s_cbranch_vccnz 8b\n"
        " 9:\n"
        ".endm\n"
        ".macro PK4 op, d, a, b, c, da, db, dc, mod:vararg\n"
        " .set e, 0\n"
        " .rept 4\n"
        "  .if \\c >= 0\n"
        "   \\op v[\\d+e:\\d+e+1], v[\\a+\\da*e:\\a+\\da*e+1], v[\\b+\\db*e:\\b+\\db*e+1], v[\\c+\\dc*e:\\c+\\dc*e+1] \\mod\n"
        "  .else\n"
        "   \\op v[\\d+e:\\d+e+1], v[\\a+\\da*e:\\a+\\da*e+1], v[\\b+\\db*e:\\b+\\db*e+1] \\mod\n"
        "  .endif\n"
        "  .set e, e+2\n"
        " .endr\n"
        ".endm\n"
        ".macro TR8 op\n"
        " .set e, 0\n"
        " .rept 8\n"
        "  \\op v[64+e], v[64+e]\n"
        "  .set e, e+1\n"
        " .endr\n"
        ".endm\n"
        ".macro HST y\n"
        " buffer_store_dwordx4 v[16*(\\y&3)+8*(\\y/4):16*(\\y&3)+8*(\\y/4)+3], v[138+\\y/4], %8, 0 offen offset:(\\y&3)*1024 sc1\n"
        ".endm\n"
        ".macro HSL n\n"
        " .if (y >= " HB ") && (yp < y) && (" HN " & \\n)\n"
        "  HST yp\n"
        "  .set yp, yp+1\n"
        " .endif\n"
        ".endm\n"
        ".macro PUBLISH n\n"
        " s_waitcnt vmcnt(\\n)\n"
        " buffer_store_dwordx2 v[68:69], v140, %8, 0 offen sc1\n"
        ".endm\n"
        ".macro G2RD k, n\n"
        " .set b, (0x63052417 >> (4*(\\k&7))) & 7\n"
        " .set ws, 144+16*(\\k%%6)\n"
        " ds_read_b64_tr_b16 v[ws+2*\\n:ws+2*\\n+1], v[240+4*(b/4)+\\n/2] offset:(b&3)*16384+(\\n&1)*2048\n"
        ".endm\n"
        ".macro G2RDS q0, cnt\n"
        " .set rq, \\q0\n"
        " .rept \\cnt\n"
        "  .set rk, rq/8\n"
        "  .set rn, rq&7\n"
        "  G2RD rk, rn\n"
        "  .set rq, rq+1\n"
        " .endr\n"
        ".endm\n"
        ".macro W2LD k\n"
        " s_add_u32 m0, %19, ((0x63052417 >> (4*(\\k&7))) & 7)*16384\n"
        " s_nop 0\n"
        " buffer_load_dwordx4 v143, %17, s[63+\\k] offen lds\n"
        " buffer_load_dwordx4 v72, %17, s[63+\\k] offen offset:1024 lds\n"
        " buffer_load_dwordx4 v73, %17, s[63+\\k] offen offset:2048 lds\n"
        " buffer_load_dwordx4 v74, %17, s[63+\\k] offen offset:3072 lds\n"
        ".endm\n"
        ".macro G2AT q\n"
        " .if \\q < 54\n"
        "  .set gk, \\q / 6\n"
        "  .set gu, \\q %% 6\n"
        " .else\n"
        "  .set gu, 2*((\\q-54)/9)\n"
        "  .set gk, 9+(\\q-54)%%9\n"
        "  .if gk > 12\n"
        "   .set gu, gu+1\n"
        "   .set gk, gk-4\n"
        "  .endif\n"
        " .endif\n"
        ".endm\n"
        ".macro G2LD q\n"
        " G2AT \\q\n"
        " buffer_load_dwordx4 v[96+4*(\\q%%12):99+4*(\\q%%12)], v[248+(gu&1)+2*(gu/4)], %8, s[52+gk] offen offset:(gu&2)*1024 sc1\n"
        ".endm\n"
        ".macro G2MF d\n"
        " .set ws, 144+16*(k%%6)+4*\\d\n"
        " .if k > 0\n"
        "  v_mfma_f32_16x16x32_bf16 v[16*u+4*\\d:16*u+4*\\d+3], v[ws:ws+3], v[96+4*(q%%12):99+4*(q%%12)], v[16*u+4*\\d:16*u+4*\\d+3]\n"
        " .else\n"
        "  v_mfma_f32_16x16x32_bf16 v[16*u+4*\\d:16*u+4*\\d+3], v[ws:ws+3], v[96+4*q:99+4*q], 0\n"
        " .endif\n"
        ".endm\n"
        ".macro G2WR n\n"
        " .set dw, " WRN "*(k-9)+\\n/2-2+" WRN "\n"
        " .if (k >= 9) && (u > 0) && (\\n&1) && (dw >= " WRN "*(k-9)) && (dw < 4)\n"
        "  .set t, u-1\n"
        "  ds_write_b128 v[252+t/4], v[16*t+4*dw:16*t+4*dw+3] offset:(t&3)*16384+dw*1024\n"
        "  .if (t&1) && (dw == 3)\n"
        "   ds_write_b32 v254, v254 offset:(%c14+4*(t/2))*4\n"
        "  .endif\n"
        " .endif\n"
        ".endm\n"
        "s_branch 20f\n"
        "3:\n"
        "LWAIT v137, %c12, 1\n"
        ".set m, 1\n"
        ".rept 8\n"
        " STRD m\n"
        " .set m, m+1\n"
        ".endr\n"
        "s_waitcnt lgkmcnt(0)\n"
        "s_branch 4f\n"
        "5:\n"
        "s_barrier\n"
        "s_branch 6f\n"
        "10:\n"
        "PUBLISH 0\n"
        "s_branch 7f\n"
        "11:\n"
        "LWAIT v254, 4, 7\n"
        "s_branch 12f\n"
        "20:\n"
        "v_mov_b32 v160, 0xbdd2d3e8\n"
        "v_mov_b32 v161, 0xc0135761\n"
        "v_mov_b32 v162, 1.0\n"
        "v_and_b32 v163, -16, v136\n"
        "s_mov_b32 %7, 2\n"
        "s_waitcnt lgkmcnt(0)\n" ASM_TS(7)
        "s_barrier\n" ASM_TS(0) ASM_TC(8)
        "ds_read_b64 v[166:167], v163 offset:%c10\n"
        ".set n, 0\n"
        ".rept 12\n"
        " G1RD n, 64, 0, 0\n"
        " .set n, n+1\n"
        ".endr\n"
        "1:\n"
        "s_waitcnt lgkmcnt(0)\n"
        "G1H 64, 96, 0, 1, 0\n"
        "s_waitcnt lgkmcnt(0)\n"
        "s_barrier\n"
        "G1H 96, 64, 1, 0, 0\n"
        "s_waitcnt lgkmcnt(0)\n"
        "G1H 64, 96, 1, 1, 0\n"
        "s_waitcnt lgkmcnt(0)\n"
        "s_barrier\n"
        "G1H 96, 64, 2, 0, 0\n"
        "s_waitcnt lgkmcnt(0)\n"
        "G1H 64, 96, 2, 1, 0\n"
        "s_waitcnt lgkmcnt(0)\n"
        "s_barrier\n"
        "s_sub_u32 %7, %7, 1\n"
        "G1H 96, 64, 0, 0, 0\n"
        "s_cbranch_scc0 1b\n"
        "s_waitcnt lgkmcnt(0)\n"
        "G1H 64, 96, 0, 1, 1\n"
        "s_waitcnt lgkmcnt(0)\n"
        "v_cmp_eq_u32 vcc, 0, v164\n"
        "s_bitcmp1_b32 %11, 0\n"
        "G1H 96, 64, -1, 0, 0\n"
        "s_setprio 2\n" ASM_TS(1) ASM_TC(9)
        "s_cbranch_scc0 5b\n"
        "s_cbranch_vccnz 3b\n"
        "4:\n" ASM_TS(2)
        ".set y, 0\n"
        ".set yp, 0\n"
        ".rept 8\n"
        " .set a, 16*(y&3)+8*(y/4)\n"
        " .set t, 168+2*(y&3)\n"
        " PK4 v_pk_fma_f32, a, t, 176+8*(y/4), a, 0, 1, 1, op_sel_hi:[0,1,1] neg_lo:[1,0,0] neg_hi:[1,0,0]\n"
        " HSL 1\n"
        " PK4 v_pk_fma_f32, a, a, t, 144+8*(y/4), 1, 0, 1, op_sel:[0,1,0]\n"
        " .if y == " B9 "\n"
        "  s_barrier\n"
        " .endif\n"
        " PK4 v_pk_mul_f32, 64, a, a, -1, 1, 1, 0\n"
        " PK4 v_pk_fma_f32, 64, 64, 160, 160, 1, 0, 0, op_sel:[0,0,1] op_sel_hi:[1,0,1]\n"
        " PK4 v_pk_mul_f32, 64, a, 64, -1, 1, 1, 0\n"
        " HSL 2\n"
        " TR8 v_exp_f32\n"
        " HSL 4\n"
        " PK4 v_pk_add_f32, 64, 64, 162, -1, 1, 0, 0, op_sel_hi:[1,0]\n"
        " HSL 8\n"
        " TR8 v_rcp_f32\n"
        " HSL 16\n"
        " PK4 v_pk_mul_f32, a, a, 64, -1, 1, 1, 0\n"
        " v_cvt_pk_bf16_f32 v[a], v[a], v[a+1]\n"
        " v_cvt_pk_bf16_f32 v[a+1], v[a+2], v[a+3]\n"
        " v_cvt_pk_bf16_f32 v[a+2], v[a+4], v[a+5]\n"
        " v_cvt_pk_bf16_f32 v[a+3], v[a+6], v[a+7]\n"
        " .set y, y+1\n"
        ".endr\n"
        ".rept 8-yp\n"
        " HST yp\n"
        " .set yp, yp+1\n"
        ".endr\n"
        "6:\n" ASM_TS(3)
        "v_mov_b64 v[68:69], v[166:167]\n"
        "s_bitcmp1_b32 %11, 1\n"
        "v_add_u32 v72, 9216, v143\n"
        "v_add_u32 v73, 2*9216, v143\n"
        "v_add_u32 v74, 3*9216, v143\n"
        "s_barrier\n"
        "s_cbranch_scc0 10b\n"
        ".set k, 5\n"
        ".rept " WB "\n"
        " W2LD k\n"
        " .set k, k+1\n"
        ".endr\n"
        "G2RDS 0, 40\n"
        "PUBLISH 4*" WB "\n" ASM_TS(4)
        ".rept 3-" WB "\n"
        " W2LD k\n"
        " .set k, k+1\n"
        ".endr\n"
        "s_waitcnt lgkmcnt(0)\n"
        "W2LD 8\n"
        "W2LD 9\n"
        "W2LD 10\n"
        "W2LD 11\n" ASM_TS(5)
        "s_mov_b32 %7, 0x8000\n"
        "8:\n"
        "buffer_load_dwordx2 v[70:71], v141, %8, 0 offen sc1\n"
        "s_sub_u32 %7, %7, 1\n"
        "s_cbranch_scc1 9f\n"
        "s_waitcnt vmcnt(0)\n"
        "v_cmp_ne_u64 vcc, v[68:69], v[70:71]\n"
        "s_mov_b64 exec, vcc\n"
        "s_cbranch_vccnz 8b\n"
        "9:\n"
        "s_mov_b64 exec, -1\n" ASM_TS(6)
        ".set q, 0\n"
        ".rept 11\n"
        " G2LD q\n"
        " .set q, q+1\n"
        ".endr\n"
        "s_setprio 0\n"
        ".set q, 0\n"
        ".rept 81\n"
        " G2AT q\n"
        " .set k, gk\n"
        " .set u, gu\n"
        " .set o, 10\n"
        " .if q > 70\n"
        "  .set o, 80-q\n"
        " .endif\n"
        " .if ((q%%6) == 0) && (q >= 6) && (q <= 54)\n"
        "  s_waitcnt vmcnt(o) lgkmcnt(0)\n"
        " .else\n"
        "  s_waitcnt vmcnt(o)\n"
        " .endif\n"
        " G2MF 0\n"
        " .set ql, q+11\n"
        " .if ql < 81\n"
        "  G2LD ql\n"
        " .endif\n"
        " G2WR 0\n"
        " G2MF 1\n"
        " .if q == 6\n"
        "  ds_write_b32 v254, v254 offset:%c13*4\n"
        " .endif\n"
        " .if q == 41\n"
        "  ds_read_b32 v255, v254 offset:16\n"
        " .endif\n"
        " .if q == 42\n"
        "  v_cmp_gt_i32 vcc, 7, v255\n"
        " .endif\n"
        " G2WR 1\n"
        " G2MF 2\n"
        " .set kr, k+5\n"
        " .if (u >= 1) && (u <= 4) && (k <= 8)\n"
        "  .set n, 2*u-2\n"
        "  G2RD kr, n\n"
        " .endif\n"
        " G2WR 2\n"
        " G2MF 3\n"
        " .if (u >= 1) && (u <= 4) && (k <= 8)\n"
        "  .set n, 2*u-1\n"
        "  G2RD kr, n\n"
        " .endif\n"
        " G2WR 3\n"
        " .if q == 42\n"
        "  s_cbranch_vccnz 11b\n"
        "  12:\n"
        "  s_setprio " M_PRIO "\n"
        " .endif\n"
        " .set q, q+1\n"
        ".endr\n"
        "s_nop 4\n"
        ".set n, 0\n .rept 4\n ds_write_b128 v253, v[80+4*n:83+4*n] offset:16384+n*1024\n .set n, n+1\n .endr\n"
        "ds_write_b32 v254, v254 offset:(%c14+8)*4\n"
        "7:\n"
        ".purgem G1RD\n .purgem STRD\n .purgem G1H\n .purgem LWAIT\n .purgem PK4\n .purgem TR8\n .purgem HST\n .purgem HSL\n .purgem PUBLISH\n"
        ".purgem G2RD\n .purgem G2RDS\n .purgem G2AT\n .purgem G2WR\n .purgem W2LD\n .purgem G2LD\n .purgem G2MF\n"
        : "+{v[0:31]}"(acc[0]), "+{v[32:63]}"(acc[1]), "+s"(zo), "+{v[128:143]}"(va), "+{v[144:159]}"(bb), "+{v[240:255]}"(vb), "+{s[52:67]}"(sh), "=&s"(cnt)
        : "s"(mkd(p.ws, WS_END)), "n"(0), "n"(L_TAG - L_FLAG), "s"(role), "n"(F_STAT), "n"(F_GELU), "n"(F_DONE), "n"(0), "n"(HS),
          "s"(mkd(p.w2, F * D * 2)), "{s[68:75]}"(sw), "s"(Lb + w * 4096)
        : "memory", "scc", "vcc", "v64", "v65", "v66", "v67", "v68", "v69", V10(7), V10(8), V10(9), V10(10), V10(11), "v120", "v121", "v122", "v123", "v124", "v125", "v126", "v127",
          V10(16), V10(17), V10(18), V10(19), V10(20), V10(21), V10(22), V10(23)
    );
  }
  if (job) ST(5);
}

constexpr int P_KT = 6 * CONV_BUF, P_KV = P_KT + 24576;
static_assert(P_KV + 3 * KV_IN <= L_FLAG, "lds");
__device__ __forceinline__ float dot2(unsigned a, unsigned b, float c) {
  return __builtin_amdgcn_fdot2_f32_bf16(__builtin_bit_cast(bf2_t, a), __builtin_bit_cast(bf2_t, b), c, false);
}
__device__ __forceinline__ float sum8(float v) {
  v += __int_as_float(__builtin_amdgcn_mov_dpp(__float_as_int(v), 0xb1, 0xf, 0xf, true));
  v += __int_as_float(__builtin_amdgcn_mov_dpp(__float_as_int(v), 0x4e, 0xf, 0xf, true));
  return v + __int_as_float(__builtin_amdgcn_mov_dpp(__float_as_int(v), 0x141, 0xf, 0xf, true));
}
__device__ __forceinline__ void prep_load(const Params& p, LAS char* L, int pp, int w, int lane) {
  const int row = lane >> 3, o = (64 * pp + 32 * (w & 1) + row) * 1280 + 7 * 128 + (((lane & 7) ^ ((row >> 1) & 2)) << 4);
  const int vo[4] = {o ^ 64, o + 9216, (o ^ 64) + 2 * 9216, o + 3 * 9216};
  const v4u rk = mkd(w >= 2 ? p.v : p.k, S * D * 2);
#pragma unroll
  for (int n = 0; n < 3; ++n) {
    if (n == 2) dma4(rk, L + P_KV + n * KV_IN + 1024 + w * 4096, vo, n * 128);
    if (w == 0) dma16(rk, L + P_KV + n * KV_IN, row >= 5 ? o - 8 * 1280 : OOB, n * 128);
    if (w == 3) dma16(mkd(p.sk, D * 8), L + P_KV + n * KV_IN + 17408, lane < 32 ? lane * 16 : OOB, (7 + n) * 512);
  }
  __builtin_amdgcn_s_barrier();
}
__device__ __forceinline__ unsigned prep_kv(const Params& p, LAS char* L, int pp, int w, int lane) {
  const int c0 = 448 + 6 * pp;
  u64 ep = ask_epoch(p);
  TS(0);
  KvPre kp;
  kv_pre(kp, L + P_KV, L + P_KT, w, lane);
  TS(6);
  asm volatile("s_waitcnt vmcnt(0) lgkmcnt(0)" ::: "memory");
  lds_post(L, F_GELU + w, 1);
  lds_wait4(L, F_GELU, lane);
  TS(7);
  asm volatile("" : "+s"(ep));
  const u64 tag = tag_of(ep);
  kv_make<3>(kp);
  TS(1);
#pragma unroll
  for (int n = 0; n < 3; ++n) kv_store<2>(p, L + P_KT + n * 8192, 2 * w, pp, 7 + n, lane);
  lds_post(L, F_RING + w, 1);
  TS(2);
  lds_wait4(L, F_DONE, lane);
  WAIT_V(0);
  set_flag(p, lane < 3 ? flag_at(FK + (28 + 4 * lane + w) * 32 + pp) : OOB, tag);
  TS(3);
#define P_NAP 20
  if (w == 3) {
    __builtin_amdgcn_s_sleep(P_NAP);
    wait_flag<2>(p, flag_at(FK + ((c0 + (lane < 32 ? 0 : 5)) >> 4) * 32 + (lane & 31)), tag);
    lds_post(L, F_STAT, 1);
  }
  lds_wait<1>(L, F_STAT, 1);
  TS(4);
  stage_u(p, L + w * CONV_BUF, u_src(c0 + w, lane));
  if (w < 2) stage_u(p, L + (4 + w) * CONV_BUF, u_src(c0 + 4 + w, lane));
  WAIT_V(0);
  lds_post(L, F_STAGED + w, 1);
  TS(5);
  return tag >> 32;
}
__device__ __forceinline__ unsigned prep_side(const Params& p, LAS char* L, int pp, int a, int lane) {
  const int c0 = 448 + 6 * pp + 3 * (a & 1);
  LAS char* Lc = L + 3 * (a & 1) * CONV_BUF;
  *(LAS int*)(L + L_FLAG + lane * 4) = 0;
  BAR();
  TS(4);
  u64 ep, dd;
  v4u hh[3];
  asm volatile(
      "s_load_dwordx2 %0, %5, 0x0\n s_load_dwordx2 %1, %6, 0x0\n s_load_dwordx4 %2, %7, 0x0\n s_load_dwordx4 %3, %7, 0x1000\n"
      "s_load_dwordx4 %4, %7, 0x2000"
      : "=&s"(ep), "=&s"(dd), "=&s"(hh[0]), "=&s"(hh[1]), "=&s"(hh[2])
      : "s"(p.ws + WS_EPOCH), "s"(p.dv + (c0 & ~1) * 2), "s"(p.hk + c0 * 4096)
      : "memory");
  SB();
  const rsrc_t rx = mk(p.x, S * D * 2);
  const int row = lane >> 3, c = lane & 7;
  v4u xr[20];
#pragma unroll
  for (int n = 0; n < 20; ++n) xr[n] = __builtin_amdgcn_raw_buffer_load_b128(rx, (64 * pp + 16 * a + row) * 1280 + c * 16 + (n % 10) * 128, (n / 10) * 8 * 1280, 0);
  SB();
  asm volatile("s_waitcnt lgkmcnt(0)" : "+s"(ep), "+s"(dd), "+s"(hh[0]), "+s"(hh[1]), "+s"(hh[2])::"memory");
  lds_post(L, F_DONE + a, 1);
  TS(3);
  const u64 tag = tag_of(ep);
  {
    float sx[2], sq[2];
#pragma unroll
    for (int h = 0; h < 2; ++h) {
      sx[h] = sq[h] = 0.f;
#pragma unroll
      for (int n = 0; n < 40; ++n) {
        const unsigned v = xr[10 * h + (n >> 2)][n & 3];
        sx[h] = dot2(v, 0x3f803f80u, sx[h]);
        sq[h] = dot2(v, v, sq[h]);
      }
      sx[h] = sum8(sx[h]);
      sq[h] = sum8(sq[h]);
    }
    const float mean = (c ? sx[1] : sx[0]) * (1.f / D);
    const float var = fmaxf((c ? sq[1] : sq[0]) * (1.f / D) - mean * mean, 0.f);
    const unsigned md = pk(mean, __builtin_amdgcn_sqrtf(rb(rb(var) + 1e-5f)));
    __builtin_amdgcn_raw_buffer_store_b64(v2u{md, (unsigned)(tag >> 32)}, mk(p.ws, WS_END), c < 2 ? WS_STAT + (64 * pp + 16 * a + 8 * c + row) * 8 : OOB, 0, SC1);
    SB();
  }
  TS(0);
  if (a < 2) {
#pragma unroll
    for (int n = 0; n < 3; ++n) stage_pads(Lc + n * CONV_BUF, lane);
    dd >>= 16 * a;
#pragma unroll
    for (int c = 0; c < 3; ++c) stage_head(Lc + c * CONV_BUF, hh[c], ((unsigned)(dd >> (16 * c)) & 0xffffu) << 16);
  }
#define P_GATE 0
  lds_wait4<1>(L, F_RING, lane);
  if (a < 2) {
#pragma unroll
    for (int n = 0; n < 3; ++n) stage_h<0>(p, Lc + n * CONV_BUF, (c0 + n) * 4096 + lane * 16);
    WAIT_V(0);
    TS(1);
#pragma unroll
    for (int n = 0; n < 3; ++n) stage_h<1>(p, Lc + n * CONV_BUF, (c0 + n) * 4096 + lane * 16);
    WAIT_V(0);
    lds_post(L, F_STAGED + 4 + a, 1);
  } else {
    touch(p.w2 + (448 * 32 + (2 * pp + a - 2) * 46 + min(lane, 45)) * 128, L + L_DUMP);
  }
  TS(2);
  return tag >> 32;
}

__device__ __forceinline__ void next_epoch(const Params& p) {
  const u64 tag = get_tag(p);
  const int lane = lane_id();
#pragma unroll 1
  for (int spin = 0; spin < SPIN_CAP; ++spin) {
    bool ok = true;
#pragma unroll
    for (int n = 0; n < 4; ++n) {
      const v2u f = __builtin_amdgcn_raw_buffer_load_b64(mk(p.ws, WS_END), flag_at(FK + (lane >> 5) * 128 + (lane & 31)), n * 256 * FSTR, SC1);
      ok &= f.y == (unsigned)(tag >> 32) && f.x == (unsigned)tag;
    }
    if (__all(ok)) break;
    asm volatile("s_sleep 8" ::: "memory");
  }
  set_flag(p, lane == 0 ? WS_EPOCH : OOB, tag);
}

__global__ __launch_bounds__(512, 2) void h3_kernel(Params p) {
  extern __shared__ __attribute__((aligned(16))) char smem[];
  LAS char* L = (LAS char*)smem;
  int lane = threadIdx.x & 63;
  const int w = __builtin_amdgcn_readfirstlane(threadIdx.x >> 6);
  const unsigned bid = blockIdx.x;
  unsigned xcd = bid & 7, r = bid >> 3;
  v4u kvp;
  const bool big = bid < 224;
  const int who = big ? w & 6 : ~w & 4;
  asm volatile("s_cmp_eq_u32 %2, 4\n s_cbranch_scc0 1f\n s_load_dwordx4 %0, %1, 0x8\n 1:\n"
               : "={s[4:7]}"(kvp)
               : "s"(__builtin_amdgcn_kernarg_segment_ptr()), "s"(who)
               : "memory", "scc");
  SB();
  code_prefetch(__builtin_amdgcn_s_getpc() & ~255ull, L + L_DUMP, r + 32 * (w - 6), lane);
  SB();
  int um, uc, hs, ds, dm, sh, bo, vm;
  asm("s_cmpk_lt_u32 %8, 224\n s_cselect_b32 %0, 28, 4\n s_cselect_b32 %1, 0, 196\n s_cselect_b32 %2, 0, 32*1280\n s_cselect_b32 %3, 32*1280-128, 0\n"
      "s_cselect_b32 %4, 0, %9\n s_cselect_b32 %5, 13, 12\n s_cselect_b32 %6, 0, %10\n s_cselect_b32 %7, 1, 2"
      : "=s"(um), "=s"(uc), "=s"(hs), "=s"(ds), "=s"(dm), "=s"(sh), "=s"(bo), "=s"(vm)
      : "s"(bid), "n"(KV_IN - 4096), "n"(P_KV - L_KV + 32768)
      : "scc");
  const int jb = um * xcd + r + uc;
  const int ko = kv_head(kvp, who, (jb & 31) * (64 * 1280) + (jb >> 5) * 128 + (w & 1) * hs, ds + 128, (int)(size_t)L + L_KV + 1024 - 32768 + bo + (w << sh),
                         dm + 4096, w & vm, lane);
  SB();
  const int rr = (r * 37) >> 8, i = 4 * (xcd >> 1) + rr, j = 7 * (xcd & 1) + (r - 7 * rr);
  LoadPre lp;
  if (who == 4 && big) kv_rest(p, L, ko, jb, w & 1, lane);
  asm volatile("" : "+s"(xcd), "+s"(r));
  const bool g1 = r < 28;
  const int jx = 28 * (xcd >> 1) + r, rng = (jx * 205) >> 10, g5 = jx - 5 * rng, G2 = 5 * (xcd & 1) + g5, t0 = min(6 * rng, 122);
  const bool job = jx < 110;
  const int rot = (ROT >> (4 * g5)) & 15;
  if (__builtin_expect(g1 && w < 4, 0)) {
    mma_waves(p, L, i, j, job, t0, G2, rot, w, lane);
    if (bid == 223 && w == 0) next_epoch(p);
    DUMP();
    __builtin_amdgcn_endpgm();
  }
  const int pp = 4 * xcd + (r - 28);
  __builtin_amdgcn_s_setprio(3);
  if (__builtin_expect(w < 4, 0)) prep_load(p, L, pp, w, lane);
  lane = lane_id();
  ConvPre cp;
  conv_pre(cp, L, L + (g1 ? L_CONV - 2 * CONV_BUF : 0) + (w >> 1) * CONV_BUF, (g1 ? 2 * jb - 2 : 448 + 6 * pp) + (w >> 1), w, lane);
  int so[4], xa = (int)(size_t)L + L_XS + xs_at(w & 3, lane);
  w2_src(so, lane);
  int s12 = g2_step(w & 3, rot, 12) * 40960 + G2 * 128, s13 = g2_step(w & 3, rot, 13) * 40960 + G2 * 128;
  asm volatile("" : "+v"(so[0]), "+v"(so[1]), "+v"(so[2]), "+v"(so[3]), "+v"(xa), "+s"(s12), "+s"(s13));
  unsigned t32;
  v2u xv[NT];
  u32x32 held;
  const v4u rw2 = mkd(p.w2, job ? F * D * 2 : 0);
  if (__builtin_expect(g1, 1)) {
    if (__builtin_expect(w < 6, 1)) gemm1_first(p, L, lp, ko, i, j, jb, w & 1, lane);
    if (__builtin_expect(w < 6, 1)) t32 = gemm1_load(p, L, lp, rw2, so, t0, G2, rot, jb, w - 4, lane);
    else t32 = gemm1_side(p, L, i, j, t0, jb, w - 6, lane);
    asm volatile(
        ".set n, 0\n .rept 4\n buffer_load_dwordx4 v[224+4*n:227+4*n], %1, %2, %3 offen\n buffer_load_dwordx4 v[240+4*n:243+4*n], %1, %2, %4 offen\n"
        " .if n < 3\n v_add_u32 %1, 8*1280, %1\n .endif\n .set n, n+1\n .endr\n"
        "s_setprio %5\n"
        : "=&{v[224:255]}"(held), "+v"(so[0])
        : "s"(rw2), "s"(s12), "s"(s13), "n"(H_PRIO)
        : "memory");
    TS(7);
#pragma unroll
    for (int u = 0; u < NT; ++u) xv[u] = at<v2u>(xa + u * 2048);
  } else {
    if (w < 4) __builtin_amdgcn_s_setprio(2);
    if (w < 4) t32 = prep_kv(p, L, pp, w, lane);
    else t32 = prep_side(p, L, pp, w - 4, lane);
    lds_wait<1>(L, F_STAGED + min(lane & 7, 5), 1);
    asm volatile("" : "={v[224:255]}"(held));
  }
#pragma unroll 1
  for (int round = 0;; ++round) {
    conv(p, cp, w, round, g1, held, t32);
    if (__builtin_expect(w >= 4 || round, 1)) break;
    cp.ba += 4 * CONV_BUF;
    cp.aa += 4 * CONV_BUF, cp.lb += 4 * CONV_BUF, cp.cw += 4 * CONV_BUF, cp.kw += 4 * CONV_BUF, cp.yr += 4 * CONV_BUF;
    cp.yo += 4 * YS;
    asm volatile("" : "+v"(cp.ba), "+v"(cp.aa), "+v"(cp.lb), "+v"(cp.cw), "+v"(cp.kw), "+v"(cp.yr), "+v"(cp.yo));
  }
  ST(4);
  if (g1 && job) {
    lane = lane_id();
    lds_wait_at<1, (F_GELU - 4) * 4>(cp.fl, 1);
#pragma unroll
    for (int n = 0; n < 8; ++n)
      *(LAS v4u*)(L + w2_block(12 + n / 4) * 16384 + (w - 4) * 4096 + (n & 3) * 1024 + lane * 16) = v4u{held[4 * n], held[4 * n + 1], held[4 * n + 2], held[4 * n + 3]};
    asm volatile("" ::: "memory");
    at<volatile int>(cp.fl) = 7;
    __builtin_amdgcn_s_setprio(F_PRIO);
    finish(p, L, xv, t0, rng, G2, w - 4, t32, lane);
  }
  DUMP();
}
}

extern "C" void kernel_launch(void* const* d_in, const int* in_sizes, int n_in, void* d_out, int out_size, void* d_ws, size_t ws_size,
                              hipStream_t stream) {
  using namespace h3;
  Params p = {};
  p.x = (const char*)d_in[0];
  p.k = (const char*)d_in[1];
  p.v = (const char*)d_in[2];
  p.sk = (const char*)d_in[3];
  p.hk = (const char*)d_in[4];
  p.dv = (const char*)d_in[5];
  p.w1 = (const char*)d_in[7];
  p.b1 = (const char*)d_in[8];
  p.w2 = (const char*)d_in[9];
  p.b2 = (const char*)d_in[10];
  p.out = (char*)d_out;
  p.ws = (char*)d_ws;
  (void)hipFuncSetAttribute((const void*)h3_kernel, hipFuncAttributeMaxDynamicSharedMemorySize, LDS_BYTES);
  h3_kernel<<<256, 512, LDS_BYTES, stream>>>(p);
}
```

```cpp
#include <hip/hip_runtime.h>
#include <stdint.h>

namespace h3 {
#define LAS __attribute__((address_space(3)))
#define GAS __attribute__((address_space(1)))
typedef unsigned v4u __attribute__((ext_vector_type(4)));
typedef unsigned v2u __attribute__((ext_vector_type(2)));
typedef float f32x4 __attribute__((ext_vector_type(4)));
typedef float f32x2 __attribute__((ext_vector_type(2)));
typedef float f32x16 __attribute__((ext_vector_type(16)));
typedef float f32x32 __attribute__((ext_vector_type(32)));
typedef unsigned u32x32 __attribute__((ext_vector_type(32)));
typedef int i32x16 __attribute__((ext_vector_type(16)));
typedef int i32x8 __attribute__((ext_vector_type(8)));
typedef short s16x8 __attribute__((ext_vector_type(8)));
typedef short s16x4 __attribute__((ext_vector_type(4)));
typedef __bf16 bf2_t __attribute__((ext_vector_type(2)));
typedef unsigned long long u64;
typedef __amdgpu_buffer_rsrc_t rsrc_t;

constexpr int S = 2048, D = 640, F = 1728, KS2 = 54, NT = 6;
constexpr int SC1 = 16;
constexpr int WS_EPOCH = 0;
constexpr int WS_STAT = 4096;
constexpr int WS_CS = 20480;
constexpr int HS = 129 * 1024;
constexpr int WS_HT = 196608;
constexpr int WS_KVT = WS_HT + KS2 * HS;
constexpr int YS = 22 * 256;
constexpr int WS_YT = WS_KVT + D * S * 2;
constexpr int WS_FL = WS_YT + D * YS;
constexpr int FSTR = 128;
constexpr int FH = 0;
constexpr int FK = 1024;
constexpr int WS_END = WS_FL + 2304 * FSTR;
__device__ __forceinline__ int flag_at(int n) { return WS_FL + n * FSTR; }
constexpr int OOB = 0x40000000;
constexpr int L_W1 = 49152;
constexpr int L_KV = 98304, L_KT = L_KV + 18432;
constexpr int L_XS = 98304;
constexpr int C_A2 = 4128, C_B = 8256, C_BS = 1184, CONV_BUF = C_B + 4 * C_BS;
constexpr int L_CONV = 131072;
constexpr int L_STAT = L_CONV + 2 * CONV_BUF, L_CS = L_STAT + 1024, L_STAT2 = L_CS + 512, L_FLAG = L_STAT2 + 768, L_DUMP = L_FLAG + 256, L_TAG = L_DUMP + 1024, L_B2 = L_TAG + 16;
constexpr int LDS_BYTES = 163840;
static_assert(L_DUMP + 2048 <= LDS_BYTES, "lds");
constexpr int F_PAIR = 0, F_STAT = 12, F_GELU = 16, F_RING = 20, F_STAGED = 24, F_DONE = 32;
#define CODE_BYTES 32512
constexpr int SPIN_CAP = 1 << 15;

struct Params {
  const char *x, *k, *v, *sk, *hk, *dv, *w1, *b1, *w2, *b2;
  char *out, *ws;
};

__device__ __forceinline__ rsrc_t mk(const void* p, int bytes) { return __builtin_amdgcn_make_buffer_rsrc((void*)p, 0, bytes, 0x00020000); }
__device__ __forceinline__ f32x4 mfma(v4u a, v4u b, f32x4 c) {
  return __builtin_amdgcn_mfma_f32_16x16x32_bf16(__builtin_bit_cast(s16x8, a), __builtin_bit_cast(s16x8, b), c, 0, 0, 0);
}
__device__ __forceinline__ v2u trread(LAS char* a) { return __builtin_bit_cast(v2u, __builtin_amdgcn_ds_read_tr16_b64_v4i16((LAS s16x4*)a)); }
__device__ __forceinline__ v4u cat(v2u a, v2u b) { return v4u{a.x, a.y, b.x, b.y}; }
__device__ __forceinline__ float blo(unsigned u) { return __uint_as_float(u << 16); }
__device__ __forceinline__ float bhi(unsigned u) { return __uint_as_float(u & 0xffff0000u); }
__device__ __forceinline__ unsigned pk(float a, float b) {
  bf2_t r;
  r[0] = (__bf16)a;
  r[1] = (__bf16)b;
  return __builtin_bit_cast(unsigned, r);
}
__device__ __forceinline__ float rb(float a) { return blo(pk(a, a)); }
__device__ __forceinline__ unsigned pks(float a, float b) {
  asm("" : "+v"(a));
  asm("" : "+v"(b));
  return pk(a, b);
}
__device__ __forceinline__ v4u mkd(const void* p, int bytes) {
  const u64 a = (u64)p;
  return v4u{(unsigned)a, (unsigned)(a >> 32) & 0xffffu, (unsigned)bytes, 0x00020000u};
}
__device__ __forceinline__ void dma16(v4u rs, LAS char* dst, int voff, int soff) {
  asm volatile("s_mov_b32 m0, %0\n\ts_nop 0\n\tbuffer_load_dwordx4 %1, %2, %3 offen lds" ::"s"((unsigned)(size_t)dst), "v"(voff), "s"(rs), "s"(soff)
               : "memory");
}
__device__ __forceinline__ void dma16c(v4u rs, LAS char* dst, int voff, int soff) {
  asm volatile("s_mov_b32 m0, %0\n\ts_nop 0\n\tbuffer_load_dwordx4 %1, %2, %3 offen sc1 lds" ::"s"((unsigned)(size_t)dst), "v"(voff), "s"(rs), "s"(soff)
               : "memory");
}
__device__ __forceinline__ void dma4(v4u rs, LAS char* dst, const int (&vo)[4], int soff) {
  asm volatile(
      "s_mov_b32 m0, %0\n\ts_nop 0\n\tbuffer_load_dwordx4 %1, %5, %6 offen lds\n\tbuffer_load_dwordx4 %2, %5, %6 offen offset:1024 lds\n\t"
      "buffer_load_dwordx4 %3, %5, %6 offen offset:2048 lds\n\tbuffer_load_dwordx4 %4, %5, %6 offen offset:3072 lds" ::"s"((unsigned)(size_t)dst),
      "v"(vo[0]), "v"(vo[1]), "v"(vo[2]), "v"(vo[3]), "s"(rs), "s"(soff)
      : "memory");
}
template <int AUX>
__device__ __forceinline__ void st128(v4u v, rsrc_t rs, int voff) { __builtin_amdgcn_raw_buffer_store_b128(v, rs, voff, 0, AUX); }
#define WAIT_V(n) asm volatile("s_waitcnt vmcnt(" #n ")" ::: "memory")
#define WAIT_L0() asm volatile("s_waitcnt lgkmcnt(0)" ::: "memory")
#define BAR()                          \
  do {                                 \
    WAIT_L0();                         \
    __builtin_amdgcn_s_barrier();      \
    asm volatile("" ::: "memory");     \
  } while (0)
#define SB() __builtin_amdgcn_sched_barrier(0)

__device__ __forceinline__ int lane_id() {
  int l;
  asm volatile("v_mbcnt_lo_u32_b32 %0, -1, 0\n\tv_mbcnt_hi_u32_b32 %0, -1, %0" : "=v"(l));
  return l;
}
template <int NAP = 0>
__device__ __forceinline__ void wait_flag(const Params& p, int off, u64 tag) {
  int cnt = SPIN_CAP;
  u64 f;
  asm volatile(
      "1:\n"
      "buffer_load_dwordx2 %0, %2, %3, 0 offen sc1\n"
      "s_sub_u32 %1, %1, 1\n"
      "s_cbranch_scc1 2f\n"
      "s_sleep %5\n"
      "s_waitcnt vmcnt(0)\n"
      "v_cmp_ne_u64 vcc, %4, %0\n"
      "s_mov_b64 exec, vcc\n"
      "s_cbranch_vccnz 1b\n"
      "2:\n"
      "s_mov_b64 exec, -1\n"
      "s_waitcnt vmcnt(0)\n"
      : "=&v"(f), "+s"(cnt)
      : "v"(off), "s"(mkd(p.ws, WS_END)), "s"(tag), "n"(NAP)
      : "memory", "vcc", "scc");
}
__device__ __forceinline__ void set_flag(const Params& p, int off, u64 tag) {
  __builtin_amdgcn_raw_buffer_store_b64(v2u{(unsigned)tag, (unsigned)(tag >> 32)}, mk(p.ws, WS_END), off, 0, SC1);
}
__device__ __forceinline__ void lds_post(LAS char* L, int idx, int val) {
  asm volatile("" ::: "memory");
  *(volatile LAS int*)(L + L_FLAG + idx * 4) = val;
}
__device__ __forceinline__ void lds_post2(LAS char* L, int idx) {
  asm volatile("" ::: "memory");
  *(volatile LAS v2u*)(L + L_FLAG + idx * 4) = v2u{1u, 1u};
}
template <int NAP = 0, int OFF = 0>
__device__ __forceinline__ void lds_wait_at(int a, int val) {
  int cnt = 1 << 22, f;
  asm volatile(
      "1:\n"
      "ds_read_b32 %0, %2 offset:%5\n"
      "s_sub_u32 %1, %1, 1\n"
      "s_cbranch_scc1 2f\n"
      ".if %4\n s_sleep %4\n .endif\n"
      "s_waitcnt lgkmcnt(0)\n"
      "v_cmp_gt_i32 vcc, %3, %0\n"
      "s_cbranch_vccnz 1b\n"
      "2:\n"
      "s_waitcnt lgkmcnt(0)\n"
      : "=&v"(f), "+s"(cnt)
      : "v"(a), "s"(val), "n"(NAP), "n"(OFF)
      : "memory", "vcc", "scc");
}
template <int NAP = 0>
__device__ __forceinline__ void lds_wait(LAS char* L, int idx, int val) { lds_wait_at<NAP>((int)(size_t)(L + L_FLAG + idx * 4), val); }
template <int NAP = 0>
__device__ __forceinline__ void lds_wait4(LAS char* L, int idx, int lane) { lds_wait<NAP>(L, idx + (lane & 3), 1); }
__device__ __forceinline__ u64 tag_of(u64 e) { return e * 6364136223846793005ull + 1442695040888963407ull; }
__device__ __forceinline__ u64 ask_epoch(const Params& p) {
  u64 e;
  asm volatile("s_load_dwordx2 %0, %1, 0x0" : "=s"(e) : "s"(p.ws + WS_EPOCH) : "memory");
  return e;
}
__device__ __forceinline__ u64 get_tag(const Params& p) {
  u64 e;
  asm volatile("s_load_dwordx2 %0, %1, 0x0\n\ts_waitcnt lgkmcnt(0)" : "=s"(e) : "s"(p.ws + WS_EPOCH) : "memory");
  return tag_of(e);
}
__device__ __forceinline__ void touch(const char* a, LAS char* dump) {
  asm volatile("s_mov_b32 m0, %0\n\ts_nop 0\n\tglobal_load_lds_dword %1, off" ::"s"((unsigned)(size_t)dump), "v"(a) : "memory");
}
__device__ __forceinline__ void code_prefetch(u64 pc, LAS char* dump, int r, int lane) {
  dma16(mkd((const void*)pc, CODE_BYTES), dump, r * 1024 + lane * 16, 0);
}

constexpr int KV_IN = 18432;
struct KvPre {
  v4u m, sf;
  int sel;
  LAS char *ka[2], *va, *sa, *ta[2];
};
__device__ __forceinline__ void kv_pre(KvPre& k, LAS char* Li, LAS char* Lt, int cb, int lane) {
  const int c = lane & 15, g = lane >> 4, tok = 4 * (c & 3) + (c >> 2);
#pragma unroll
  for (int d = 0; d < 4; ++d) k.m[d] = (g & 1) == (c >> 3) && d == ((c >> 1) & 3) ? (c & 1 ? 0xffff0000u : 0xffffu) : 0u;
  k.sf = k.m & ((g >> 1) == (cb & 1) ? 0x3f803f80u : 0u);
  k.sel = g & 2 ? 0x03020302 : 0x01000100;
#pragma unroll
  for (int y = 0; y < 2; ++y) {
    const int row = 8 + tok - 2 * y - (g >> 1);
    k.ka[y] = Li + row * 128 + (((2 * cb + (g & 1)) ^ ((row >> 1) & 6)) << 4);
  }
  k.va = Li + 9216 + tok * 128 + (((4 * (cb >> 1) + g) ^ 4 ^ ((tok >> 1) & 6)) << 4);
  k.sa = Li + 17408 + cb * 128 + c * 8;
  k.ta[0] = Lt + (16 * cb + c) * 128 + (((2 * g) ^ (c & 7)) << 4);
  k.ta[1] = Lt + (16 * cb + c) * 128 + (((2 * g + 1) ^ (c & 7)) << 4);
  asm volatile("" : "+v"(k.m), "+v"(k.sf), "+v"(k.sel), "+v"(k.ka[0]), "+v"(k.ka[1]), "+v"(k.va), "+v"(k.sa), "+v"(k.ta[0]), "+v"(k.ta[1]));
}
template <int N>
__device__ __forceinline__ void kv_make(const KvPre& k) {
  v2u sk[N];
  v4u kf[N][4][2], vf[N][4];
#pragma unroll
  for (int n = 0; n < N; ++n) {
    sk[n] = *(LAS v2u*)(k.sa + n * KV_IN);
#pragma unroll
    for (int q = 0; q < 4; ++q) {
      vf[n][q] = *(LAS v4u*)(k.va + n * KV_IN + q * 2048);
#pragma unroll
      for (int y = 0; y < 2; ++y) kf[n][q][y] = *(LAS v4u*)(k.ka[y] + n * KV_IN + q * 2048);
    }
  }
  SB();
#pragma unroll
  for (int n = 0; n < N; ++n) {
    const v4u w0 = k.m & __builtin_amdgcn_perm(sk[n].x, sk[n].x, k.sel), w1 = k.m & __builtin_amdgcn_perm(sk[n].y, sk[n].y, k.sel);
    const f32x4 Z = {0.f, 0.f, 0.f, 0.f};
    v2u o2[4];
#pragma unroll
    for (int q = 0; q < 4; ++q) {
      const f32x4 o = mfma(kf[n][q][1], w1, mfma(kf[n][q][0], w0, Z)) * mfma(vf[n][q], k.sf, Z);
      o2[q] = v2u{pk(o[0], o[1]), pk(o[2], o[3])};
    }
    *(LAS v4u*)(k.ta[0] + n * 8192) = cat(o2[0], o2[1]);
    *(LAS v4u*)(k.ta[1] + n * 8192) = cat(o2[2], o2[3]);
  }
}
template <int N>
__device__ __forceinline__ void kv_store(const Params& p, LAS char* Lt, int n0, int pp, int G, int lane) {
  const int row = lane >> 3;
#pragma unroll
  for (int n = 0; n < N; ++n)
    st128<SC1>(*(LAS v4u*)(Lt + (n0 + n) * 1024 + lane * 16), mk(p.ws, WS_END),
               WS_KVT + (64 * G + 8 * (n0 + n) + row) * 4096 + 128 * pp + (((lane & 7) ^ row) << 4));
}

__device__ __forceinline__ int g2_step(int w, int rot, int k) {
  return k == 13 ? 26 + 27 * (w >> 1) : 27 * (w >> 1) + 13 * (w & 1) + (k + rot >= 13 ? k + rot - 13 : k + rot);
}
__device__ __forceinline__ int w2_block(int k) { return (0x63052417u >> (4 * (k & 7))) & 7; }
__device__ __forceinline__ void w2_src(int (&so)[4], int lane) {
  const int row = lane >> 3;
#pragma unroll
  for (int h = 0; h < 4; ++h) so[h] = (8 * h + row) * 1280 - 1024 * h + (((lane & 7) ^ (((row >> 1) & 3) << 1)) << 4);
}
__device__ __forceinline__ void w2_fetch(v4u rw2, LAS char* L, const int (&so)[4], int w, int rot, int k, int G2) {
  dma4(rw2, L + w2_block(k) * 16384 + w * 4096, so, g2_step(w, rot, k) * (32 * 1280) + G2 * 128);
}

__device__ __forceinline__ void stage_pads(LAS char* Lc, int lane) {
  if (lane < 16) *(LAS unsigned*)(Lc + (lane >> 3) * C_A2 + (lane & 7) * 4) = 0u;
  *(LAS v2u*)(Lc + C_B + (lane >> 4) * C_BS + (lane & 15) * 8) = v2u{0u, 0u};
}
template <int DELAYED>
__device__ __forceinline__ void stage_h(const Params& p, LAS char* Lc, int o) {
  asm volatile(
      "s_mov_b32 m0, %0\n\ts_mov_b64 exec, -2\n\tbuffer_load_dwordx4 %1, %2, 0 offen lds\n\ts_mov_b64 exec, -1\n\t"
      "buffer_load_dwordx4 %1, %2, 0 offen offset:1024 lds\n\tbuffer_load_dwordx4 %1, %2, 0 offen offset:2048 lds\n\t"
      "buffer_load_dwordx4 %1, %2, 0 offen offset:3072 lds" ::"s"((unsigned)(size_t)(Lc + DELAYED * C_A2 + 32)),
      "v"(o), "s"(mkd(p.hk - 4 * DELAYED, D * S * 2 + 4 * DELAYED))
      : "memory");
}
__device__ __forceinline__ int u_src(int ch, int lane) { return WS_KVT + ch * 4096 + (lane >> 3) * 512 + (lane & 7) * 16; }
__device__ __forceinline__ void stage_u(const Params& p, LAS char* Lc, int o) {
  asm volatile(
      "s_mov_b32 m0, %0\n s_nop 0\n"
      ".set e, 0\n .rept 4\n buffer_load_dwordx4 %1, %2, 0 offen offset:e*128 sc1 lds\n"
      " .if e < 3\n s_add_u32 m0, m0, %3\n s_nop 0\n .endif\n .set e, e+1\n .endr\n" ::"s"((unsigned)(size_t)(Lc + C_B + 128)),
      "v"(o), "s"(mkd(p.ws, WS_END)), "n"(C_BS - 128)
      : "memory", "scc");
}
__device__ __forceinline__ void stage_head(LAS char* Lc, v4u h, unsigned dbits) {
  const unsigned h01 = pk(blo(h.x) + __uint_as_float(dbits), bhi(h.x));
  *(LAS v4u*)(Lc + 32) = v4u{h01, h.y, h.z, h.w};
  *(LAS v4u*)(Lc + C_A2 + 32) = v4u{0u, h01, h.y, h.z};
}
#define OUT_AUX 0
#define H_PRIO 0
#define L_NB 3
#define ROT 0
#define E_YST 3
#define RI 1
#define RJ 1
__device__ __forceinline__ int rot_of(int i, int j) { return (RI * i + RJ * j) % 10; }
__device__ __forceinline__ int kv_head(v4u kv, int who, int so, int ds, int dst, int dm, int v, int lane) {
  const int o = (lane >> 3) * 1280 + (((lane << 4) & 0x70) ^ (lane & 32));
  asm volatile(
      "s_cmp_eq_u32 %9, 4\n s_cbranch_scc0 1f\n s_setprio 3\n"
      "s_mov_b32 m0, %6\n s_cmp_eq_u32 %7, 0\n s_waitcnt lgkmcnt(0)\n"
      "s_cselect_b64 s[4:5], s[4:5], s[6:7]\n s_mov_b32 s6, %8\n s_mov_b32 s7, 0x20000\n s_and_b32 s5, s5, 0xffff\n"
      ".set h, 0\n .rept 2\n"
      " buffer_load_dwordx4 %2, s[4:7], %1 offen lds\n buffer_load_dwordx4 %3, s[4:7], %1 offen offset:1024 lds\n"
      " buffer_load_dwordx4 %4, s[4:7], %1 offen offset:2048 lds\n buffer_load_dwordx4 %5, s[4:7], %1 offen offset:3072 lds\n"
      " .if h == 0\n s_add_u32 m0, m0, %10\n s_add_u32 %1, %1, %11\n .endif\n .set h, 1\n"
      ".endr\n"
      "1:\n"
      : "+{s[4:7]}"(kv), "+&s"(so)
      : "v"(o ^ 64), "v"(o + 9216), "v"((o ^ 64) + 2 * 9216), "v"(o + 3 * 9216), "s"(dst), "s"(v), "n"(S * D * 2), "s"(who), "s"(dm), "s"(ds)
      : "memory", "scc");
  return o + so - 40 * 1280;
}
__device__ __forceinline__ void kv_rest(const Params& p, LAS char* L, int o, int jb, int lw, int lane) {
  int a = (jb >> 5) * 512 + lane * 16, b = lane >= 40 ? o : OOB;
  asm("" : "+v"(a), "+v"(b));
  dma16(mkd(lw ? p.sk : p.k, lw ? D * 8 : S * D * 2), L + L_KV + lw * 17408, lw ? a : b, 0);
}
struct LoadPre {
  int xo[4], wo[4], kst;
};
__device__ __forceinline__ void gemm1_first(const Params& p, LAS char* L, LoadPre& q, int ko, int i, int j, int jb, int lw, int lane) {
  SB();
  const int i16 = lane & 15, g = lane >> 4;
#pragma unroll
  for (int e = 0; e < 4; ++e) q.xo[e] = (128 * i + 64 * lw + 8 * e + (lane >> 3)) * 1280 - 1024 * e + (((lane & 7) ^ (4 * (e & 1) + g)) << 4);
#define KST i + (j >> 1)
  const int ij = KST;
  q.kst = ij - 10 * ((ij * 205) >> 11);
#pragma unroll
  for (int h = 0; h < 2; ++h) dma4(mkd(p.x, S * D * 2), L + lw * 8192 + h * 4096, q.xo, q.kst * 128 + h * 32 * 1280);
  SB();
#pragma unroll
  for (int e = 0; e < 4; ++e) q.wo[e] = (32 * lw + 4 * e + g) * 3456 - 1024 * e + 256 * j + ((i16 ^ ((g | ((e >> 1) << 2)) << 1)) << 4);
#pragma unroll
  for (int h = 0; h < 2; ++h) dma4(mkd(p.w1, D * F * 2), L + L_W1 + lw * 8192 + h * 4096, q.wo, q.kst * (64 * 3456) + h * 16 * 3456);
}
__device__ __forceinline__ unsigned gemm1_load(const Params& p, LAS char* L, const LoadPre& q, v4u rw2, const int (&so)[4], int t0, int G2, int rot, int jb, int lw,
                                               int lane) {
  int ho = (2 * jb + lw) * 4096 + lane * 16;
  int xs = (16 * t0 + 48 * lw + (lane >> 3)) * 1280 + G2 * 128 + (((lane & 7) ^ (lane >> 3)) << 4);
  asm volatile("" : "+v"(ho), "+v"(xs));
  const v4u rx = mkd(p.x, S * D * 2), rw = mkd(p.w1, D * F * 2);
  {
    const int k1 = q.kst == 9 ? 0 : q.kst + 1, mb = (int)(size_t)L + lw * 8192;
    int sx = k1 * 128, sw = k1 * (64 * 3456), sm = mb + 16384, cnt = 9, t0, t1, t2;
#define LD4(a, b, c, d, r, s)                                                                                            \
  "buffer_load_dwordx4 %" #a ", %" #r ", %" #s " offen lds\n buffer_load_dwordx4 %" #b ", %" #r ", %" #s " offen offset:1024 lds\n" \
  "buffer_load_dwordx4 %" #c ", %" #r ", %" #s " offen offset:2048 lds\n buffer_load_dwordx4 %" #d ", %" #r ", %" #s " offen offset:3072 lds\n"
    asm volatile(
        "s_branch 2f\n"
        "1:\n" LD4(8, 9, 10, 11, 16, 4)
        "s_add_u32 m0, %6, 49152\n"
        "s_nop 0\n" LD4(12, 13, 14, 15, 17, 5)
        "2:\n"
        "s_mov_b32 m0, %2\n"
        "s_sub_u32 %3, %3, 1\n"
        "s_waitcnt vmcnt(16)\n"
        "s_barrier\n"
        "s_cbranch_scc1 3f\n"
        "4:\n" LD4(8, 9, 10, 11, 16, 0)
        "s_add_u32 m0, %2, 49152\n"
        "s_add_u32 %4, %0, 32*1280\n" LD4(12, 13, 14, 15, 17, 1)
        "s_add_u32 %6, %2, 4096\n"
        "s_mov_b32 m0, %6\n"
        "s_add_u32 %5, %1, 16*3456\n"
        "s_add_u32 %0, %0, 128\n"
        "s_add_u32 %1, %1, 64*3456\n"
        "s_cmp_eq_u32 %0, 1280\n"
        "s_cselect_b32 %0, 0, %0\n"
        "s_cselect_b32 %1, 0, %1\n"
        "s_add_u32 %2, %2, 16384\n"
        "s_cmp_eq_u32 %2, %7\n"
        "s_cselect_b32 %2, %18, %2\n"
        "s_branch 1b\n"
        "3:\n"
        : "+&s"(sx), "+&s"(sw), "+&s"(sm), "+&s"(cnt), "=&s"(t0), "=&s"(t1), "=&s"(t2)
        : "s"(mb + 49152), "v"(q.xo[0]), "v"(q.xo[1]), "v"(q.xo[2]), "v"(q.xo[3]), "v"(q.wo[0]), "v"(q.wo[1]), "v"(q.wo[2]), "v"(q.wo[3]), "s"(rx),
          "s"(rw), "s"(mb)
        : "memory", "scc"
        );
  }
  v2u tg = *(volatile LAS v2u*)(L + L_TAG);
  asm volatile("s_waitcnt vmcnt(0)\n s_barrier" : "+v"(tg)::"memory");
  stage_h<1>(p, L + L_CONV + lw * CONV_BUF, ho);
#pragma unroll
  for (int h = 0; h < 6; ++h) dma16(rx, L + L_XS + (6 * lw + h) * 1024, xs, h * 8 * 1280);
#pragma unroll
  for (int k = 0; k < 5; ++k) {
    if (k == L_NB) {
      asm volatile("s_waitcnt vmcnt(%0)\n s_barrier" ::"n"(8 * L_NB) : "memory");
    }
    w2_fetch(rw2, L, so, 2 * lw, rot, k, G2);
    w2_fetch(rw2, L, so, 2 * lw + 1, rot, k, G2);
  }
  return __builtin_amdgcn_readfirstlane(tg.y);
}

__device__ __forceinline__ unsigned gemm1_side(const Params& p, LAS char* L, int i, int j, int t0, int jb, int sw, int lane) {
  LAS char* Lc = L + L_CONV + sw * CONV_BUF;
  const int ch = 2 * jb + sw;
  if (sw) *(LAS int*)(L + L_FLAG + lane * 4) = 0;
  stage_pads(Lc, lane);
  u64 ep;
  unsigned db;
  v4u hh;
  asm volatile("s_load_dwordx2 %0, %3, 0x0\n s_load_dword %1, %4, 0x0\n s_load_dwordx4 %2, %5, 0x0"
               : "=&s"(ep), "=&s"(db), "=&s"(hh)
               : "s"(p.ws + WS_EPOCH), "s"(p.dv + jb * 4), "s"(p.hk + ch * 4096)
               : "memory");
  int g = lane >> 4, q = (lane >> 2) & 3;
  const char* tch = p.w2 + jb * 8192 + lane * 128;
  int o0 = WS_STAT + (128 * i + 64 * sw + lane) * 8, o1 = WS_CS + (128 * j + 64 * sw + lane) * 8;
  int o2 = WS_STAT + (16 * t0 + 48 * sw + min(lane, 47)) * 8, o3 = sw || lane >= 32 ? OOB : flag_at(FK + (ch >> 4) * 32 + lane);
  int ho = sw ? jb * 8192 + lane * 16 : u_src(ch, lane), ho2 = ho + 4096;
  LAS char *d0 = L + L_STAT + (64 * sw + lane) * 8, *d1 = L + L_CS + (64 * sw + lane) * 4;
  LAS char* d2 = L + L_STAT2 + (48 * sw + min(lane, 47)) * 8;
  int of = lane < 2 ? flag_at(FK + (4 * (jb >> 5) + 2 * sw + lane) * 32 + (jb & 31)) : OOB;
  int oc = i < 10 && !(i & 1) ? o1 + (i >> 1) * 14336 : OOB;
  asm volatile("" : "+v"(tch), "+v"(o0), "+v"(o1), "+v"(o2), "+v"(o3), "+v"(ho), "+v"(ho2), "+v"(d0), "+v"(d1), "+v"(d2), "+v"(of), "+v"(oc));
  asm volatile("s_barrier\n s_waitcnt lgkmcnt(0)" : "+s"(ep), "+s"(db), "+s"(hh)::"memory");
  lane = lane_id();
  g = lane >> 4, q = (lane >> 2) & 3;
  int ko[4], ca[4], ka = (int)(size_t)L + L_KT + sw * 4096 + lane * 16;
  v4u one[4];
#pragma unroll
  for (int u = 0; u < 4; ++u) {
    ko[u] = WS_KVT + (64 * (jb >> 5) + 32 * sw + 8 * u + (lane >> 3)) * 4096 + 128 * (jb & 31) + (((lane & 7) ^ (lane >> 3)) << 4);
    ca[u] = (int)(size_t)L + L_W1 + (8 * g + q) * 256 + (((4 * sw + u) ^ (q | ((g & 1) << 2))) << 5) + 8 * (lane & 3);
    const unsigned o = q == u ? 0x3f803f80u : 0u;
    one[u] = v4u{o, o, o, o};
    asm volatile("" : "+v"(ko[u]), "+v"(ca[u]), "+v"(one[u]));
  }
  asm volatile("" : "+v"(ka));
  stage_head(Lc, hh, sw ? db & 0xffff0000u : db << 16);
  const u64 tag = tag_of(ep);
  const unsigned t32 = (unsigned)(tag >> 32);
  *(LAS v2u*)(L + L_TAG) = v2u{(unsigned)tag, t32};
  const v4u rwd = mkd(p.ws, WS_END);
  BAR();
  {
    v4u sa[2][4][2], kt[4];
    auto rd = [&](int s) {
#pragma unroll
      for (int u = 0; u < 4; ++u)
#pragma unroll
        for (int ks = 0; ks < 2; ++ks) {
          LAS char* aa = (LAS char*)(size_t)ca[u] + s * 16384 + ks * 8192;
          sa[s][u][ks] = cat(trread(aa), trread(aa + 1024));
        }
      SB();
    };
    f32x4 col = {0.f, 0.f, 0.f, 0.f};
    auto mm = [&](int s) {
#pragma unroll
      for (int u = 0; u < 4; ++u)
#pragma unroll
        for (int ks = 0; ks < 2; ++ks) col = mfma(one[u], sa[s][u][ks], col);
      SB();
    };
#pragma unroll
    for (int u = 0; u < 4; ++u) kt[u] = *(LAS v4u*)(size_t)(ka + u * 1024);
    rd(0);
    BAR();
    SB();
    rd(1);
#pragma unroll
    for (int u = 0; u < 4; ++u) st128<SC1>(kt[u], mk(p.ws, WS_END), ko[u]);
    SB();
    BAR();
    SB();
    mm(0);
    mm(1);
    asm volatile("s_barrier\n"
                 "s_waitcnt vmcnt(0)" : "+v"(col)::"memory");
    set_flag(p, of, tag);
    __builtin_amdgcn_raw_buffer_store_b64(v2u{__float_as_uint(col[0]), t32}, mk(p.ws, WS_END), oc, 0, SC1);
    SB();
  }
  v2u g0, g2, g1[5];
  u64 fk;
#define S_FETCH                                                                                                        \
  "buffer_load_dwordx2 %0, %10, %18, 0 offen sc1\n buffer_load_dwordx2 %1, %11, %18, 0 offen sc1\n"                    \
  "buffer_load_dwordx2 %2, %12, %18, 0 offen sc1\n buffer_load_dwordx2 %3, %13, %18, 0 offen sc1\n"                    \
  "buffer_load_dwordx2 %4, %14, %18, 0 offen sc1\n buffer_load_dwordx2 %5, %15, %18, 0 offen sc1\n"                    \
  "buffer_load_dwordx2 %6, %16, %18, 0 offen sc1\n buffer_load_dwordx2 %7, %17, %18, 0 offen sc1\n"
#define S_OUT "=&v"(g0), "=&v"(g1[0]), "=&v"(g1[1]), "=&v"(g1[2]), "=&v"(g1[3]), "=&v"(g1[4]), "=&v"(g2), "=&v"(fk)
#define S_IN "v"(o0), "v"(o1), "v"(o1 + 14336), "v"(o1 + 2 * 14336), "v"(o1 + 3 * 14336), "v"(o1 + 4 * 14336), "v"(o2), "v"(o3), "s"(rwd)
  asm volatile(
      "s_barrier\n"
      "s_barrier\n s_barrier\n s_barrier\n"
      S_FETCH
      "s_cmp_eq_u32 %8, 0\n"
      "s_mov_b32 m0, %9\n"
      "s_barrier\n"
      "s_cbranch_scc1 1f\n"
      ".set c, 0\n .rept 2\n"
      " s_mov_b64 exec, -2\n buffer_load_dwordx4 %19, %21, 0 offen lds\n s_mov_b64 exec, -1\n"
      " buffer_load_dwordx4 %19, %21, 0 offen offset:1024 lds\n buffer_load_dwordx4 %19, %21, 0 offen offset:2048 lds\n"
      " buffer_load_dwordx4 %19, %21, 0 offen offset:3072 lds\n"
      " s_add_u32 m0, m0, %24+c*(%25-%24)\n v_mov_b32 %19, %20\n"
      " .set c, c+1\n"
      ".endr\n"
      "global_load_lds_dword %22, off\n"
      "s_waitcnt vmcnt(9)\n"
      "v_mov_b64 %7, %23\n"
      "s_branch 2f\n"
      "1:\n"
      "s_waitcnt vmcnt(0)\n"
      "v_cmp_ne_u64 vcc, %23, %7\n"
      "s_cmp_lg_u32 vcc_lo, 0\n"
      "s_cbranch_scc1 2f\n"
      ".set e, 0\n .rept 8\n"
      " .if e < 4\n buffer_load_dwordx4 %19, %18, 0 offen offset:e*128 sc1 lds\n .else\n buffer_load_dwordx4 %20, %18, 0 offen offset:(e-4)*128 sc1 lds\n .endif\n"
      " .if e == 3\n s_add_u32 m0, m0, %24-3*%26\n s_nop 0\n .elseif e < 7\n s_add_u32 m0, m0, %26\n s_nop 0\n .endif\n"
      " .set e, e+1\n"
      ".endr\n"
      "2:\n"
      : S_OUT
      : "s"(sw), "s"((unsigned)(size_t)(L + L_CONV + (sw ? 32 : C_B + 128))), S_IN, "v"(ho), "v"(ho2), "s"(mkd(p.hk, D * S * 2)), "v"(tch), "s"(tag),
        "n"(CONV_BUF), "n"(L_DUMP - L_CONV - CONV_BUF - 32), "n"(C_BS - 128)
      : "memory", "scc", "vcc");
  const bool kv_there = (unsigned)__builtin_amdgcn_ballot_w64(fk != tag) == 0;
  auto stale = [&]() {
    unsigned a = g0.y & g2.y, o = g0.y | g2.y;
#pragma unroll
    for (int n = 0; n < 5; ++n) {
      a &= g1[n].y;
      o |= g1[n].y;
    }
    return __builtin_amdgcn_ballot_w64(a != t32 || o != t32) != 0;
  };
  f32x2 st0, st2;
  float cs;
  auto digest = [&]() {
    cs = __uint_as_float(g1[0].x);
#pragma unroll
    for (int n = 1; n < 5; ++n) cs += __uint_as_float(g1[n].x);
    st0 = f32x2{blo(g0.x), __builtin_amdgcn_rcpf(bhi(g0.x))};
    st2 = f32x2{blo(g2.x), __builtin_amdgcn_rcpf(bhi(g2.x))};
  };
  const bool late = stale();
  digest();
  asm volatile("s_barrier" : "+v"(st0), "+v"(st2), "+v"(cs)::"memory");
  if (__builtin_expect(late, 0)) {
    int spin = SPIN_CAP;
#pragma unroll 1
    do asm volatile(S_FETCH "s_waitcnt vmcnt(0)" : S_OUT : "n"(0), "n"(0), S_IN : "memory");
    while (stale() && --spin);
    digest();
  }
  *(LAS f32x2*)d0 = st0;
  *(LAS float*)d1 = cs;
  lds_post2(L, F_STAT + 2 * sw);
  *(LAS f32x2*)d2 = st2;
  if (__builtin_expect(!kv_there, 0)) {
    wait_flag<2>(p, flag_at(FK + (ch >> 4) * 32 + (lane_id() & 31)), tag);
    stage_u(p, L + L_CONV, ho);
    stage_u(p, L + L_CONV + CONV_BUF, ho2);
  }
  asm volatile("s_waitcnt vmcnt(0) lgkmcnt(0)" ::: "memory");
  asm volatile("s_barrier" ::: "memory");
  return t32;
}

struct ConvPre {
  v4u ba;
  int aa, fl, lb, cw, kw, yr, yo;
};
__device__ __forceinline__ void conv_pre(ConvPre& c, LAS char* L, LAS char* Lc, int ch, int me, int lane) {
  const int pw = me & 1, col = lane & 15, g = lane >> 4, b = (int)(size_t)Lc;
  c.aa = b + pw * C_A2 + 32 + 64 * g - 8 * ((lane >> 2) & 3) + 8 * (lane & 3);
#pragma unroll
  for (int e = 0; e < 4; ++e) {
    const int u = 7 + col - 2 * g + 8 * (e >> 1) + (e & 1);
    c.ba[e] = b + C_B + pw * 64 + ((u >> 2) & 3) * C_BS + (u >> 4) * 128 + (u & 3) * 8;
  }
  c.fl = (int)(size_t)L + L_FLAG + (F_PAIR + me) * 4;
  c.lb = b + lane * 16;
  const int Q = (4 * col + g + 1) & 63;
  c.cw = b + C_B + pw * 2304 + (Q & 3) * 256 + (Q >> 2) * 16;
  c.kw = b + 2048 + pw * 2048 + 32 * col + 8 * g;
  c.yr = b + 2048 + (11 * pw + g) * 192 + 8 * col;
  c.yo = WS_YT + ch * YS + pw * 11 * 256 + lane * 16;
  asm volatile("" : "+v"(c.ba), "+v"(c.aa), "+v"(c.fl), "+v"(c.lb), "+v"(c.cw), "+v"(c.kw), "+v"(c.yr), "+v"(c.yo));
}
template <typename T>
__device__ __forceinline__ LAS T& at(int a) { return *(LAS T*)(size_t)a; }
template <int H>
__device__ __forceinline__ void conv_send(const ConvPre& cp, const f32x32& z0) {
#pragma unroll
  for (int c = 4 * H; c < 4 * H + 4; ++c) at<f32x4>(cp.lb + c * 1024) = f32x4{z0[4 * c], z0[4 * c + 1], z0[4 * c + 2], z0[4 * c + 3]};
  asm volatile("; half %0" ::"n"(H) : "memory");
}
template <int H>
__device__ __forceinline__ void conv_keep(const ConvPre& cp, const f32x32& z0) {
  f32x4 y[4];
  const f32x4 c0 = at<f32x4>(cp.lb + C_B + H * 1024), c1 = at<f32x4>(cp.lb + C_B + 2304 + H * 1024);
#pragma unroll
  for (int c = 0; c < 4; ++c) y[c] = at<f32x4>(cp.lb + (4 * H + c) * 1024);
  SB();
#pragma unroll
  for (int c = 0; c < 4; ++c) {
    const int b = 16 * H + 4 * c;
    at<v2u>(cp.kw + 512 * c) = v2u{pks(y[c][0] + z0[b] + c0[c] + c1[c], y[c][1] + z0[b + 1]), pks(y[c][2] + z0[b + 2], y[c][3] + z0[b + 3])};
  }
  asm volatile("; half %0" ::"n"(H) : "memory");
}
__device__ __forceinline__ void pair_sync(const ConvPre& cp, int val) {
  asm volatile("" ::: "memory");
  at<volatile int>(cp.fl) = val;
  lds_wait_at(cp.fl ^ 4, val);
}
__device__ __forceinline__ void conv(const Params& p, const ConvPre& cp, int me, int round, bool g1, u32x32& held, unsigned t32) {
  const int pw = me & 1;
  u32x32 a0, a1, b0, b1;
  asm volatile(
      ".set n, 0\n"
      ".rept 16\n"
      " ds_read_b64_tr_b16 v[4*n:4*n+1], %4 offset:256*n+32\n"
      " ds_read_b64_tr_b16 v[4*n+2:4*n+3], %4 offset:256*n\n"
      " ds_read_b64 v[64+4*n:65+4*n], v[192+2*(n&1)] offset:32+128*(n/2)\n"
      " ds_read_b64 v[66+4*n:67+4*n], v[193+2*(n&1)] offset:32+128*(n/2)\n"
      " .set n, n+1\n"
      ".endr\n"
      : "=&{v[0:31]}"(a0), "=&{v[32:63]}"(a1), "=&{v[64:95]}"(b0), "=&{v[96:127]}"(b1)
      : "v"(cp.aa), "{v[192:195]}"(cp.ba)
      : "memory");
  if (__builtin_expect(g1, 1)) {
    asm volatile("s_waitcnt vmcnt(8) lgkmcnt(0)" ::: "memory");
    asm volatile("s_barrier" ::: "memory");
  } else if (me >= 4) {
    int a = cp.fl;
    asm volatile("" : "+v"(a));
    lds_wait_at<2>(a - 16, 2);
  }
  f32x32 z1, z0;
  asm volatile(
      "s_waitcnt lgkmcnt(0)\n"
      "s_setprio 1\n"
      ".set m, 0\n"
      ".set r, 0\n"
      ".set y, 0\n"
      ".rept 2\n"
      " .set c, 7\n"
      " .rept 8\n"
      "  .set n, 0\n"
      "  .rept 2*c+2\n"
      "   .if m == 72\n"
      "    s_waitcnt lgkmcnt(4)\n"
      "   .endif\n"
      "   .if m == 84\n"
      "    s_waitcnt lgkmcnt(0)\n"
      "    ds_write_b32 %8, %9\n"
      "   .endif\n"
      "   .set z, 128+32*(m/72)+4*c\n"
      "   .set b, 64+4*(2*c+1-n)\n"
      "   .if (m < 72) && (n == 0)\n"
      "    v_mfma_f32_16x16x32_bf16 v[z:z+3], v[0:3], v[b:b+3], 0\n"
      "   .else\n"
      "    v_mfma_f32_16x16x32_bf16 v[z:z+3], v[4*n:4*n+3], v[b:b+3], v[z:z+3]\n"
      "   .endif\n"
      "   .set cr, 7-r/4\n"
      "   .set cy, 7-y/4\n"
      "   .if (r < 32) && ((m >= 72) || (c < cr) || ((c == cr) && (n >= 2)))\n"
      "    .set b, 2*cr+1-(r&3)/2\n"
      "    ds_read_b64 v[64+4*b+2*(r&1):65+4*b+2*(r&1)], v[192+2*(b&1)+(r&1)] offset:128*(b/2)\n"
      "    .set r, r+1\n"
      "   .elseif (y < 32) && (m >= 80-cy*(cy+1))\n"
      "    .if y&3\n"
      "     v_mov_b32 v[160+4*cy+(y&3)], v[127+4*cy+(y&3)]\n"
      "    .else\n"
      "     v_mov_b32 v[160+4*cy], 0\n"
      "    .endif\n"
      "    .set y, y+1\n"
      "   .endif\n"
      "   .set m, m+1\n"
      "   .set n, n+1\n"
      "  .endr\n"
      "  .set c, c-1\n"
      " .endr\n"
      ".endr\n"
      "s_setprio 3\n"
      "s_waitcnt vmcnt(0)\n"
      "s_nop 5\n"
      : "=&{v[128:159]}"(z1), "=&{v[160:191]}"(z0), "+{v[64:95]}"(b0), "+{v[96:127]}"(b1), "+{v[224:255]}"(held)
      : "{v[0:31]}"(a0), "{v[32:63]}"(a1), "{v[192:195]}"(cp.ba), "v"(cp.fl), "v"(2 + 8 * round)
      : "memory");
  const int lane = lane_id();
  lds_wait_at(cp.fl ^ 4, 2 + 8 * round);
  {
    const bool last = lane == 63;
    at<f32x4>(cp.cw) = f32x4{last ? 0.f : z1[3], last ? z1[3] : z1[7], last ? z1[7] : z1[11], last ? z1[11] : z1[15]};
    at<f32x4>(cp.cw + 1024) = f32x4{last ? z1[15] : z1[19], last ? z1[19] : z1[23], last ? z1[23] : z1[27], last ? z1[27] : z1[31]};
  }
  if (pw) conv_send<0>(cp, z0);
  else conv_send<1>(cp, z0);
  pair_sync(cp, 3 + 8 * round);
  if (pw) conv_keep<1>(cp, z0);
  else conv_keep<0>(cp, z0);
  int r2 = cp.yr - (lane & (32 * pw)) * 4, o2 = lane < 48 ? cp.yo + 2048 : OOB;
  asm volatile("" : "+v"(r2), "+v"(o2));
  pair_sync(cp, 4 + 8 * round);
  v2u ya[3], yb[3];
#pragma unroll
  for (int n = 0; n < 3; ++n) {
    LAS char* a = (LAS char*)(size_t)(n < 2 ? cp.yr : r2) + n * 768;
    ya[n] = trread(a);
    yb[n] = trread(a + 128);
  }
  SB();
#pragma unroll
  for (int n = 0; n < E_YST; ++n) st128<SC1>(v4u{ya[n].x, ya[n].y, yb[n].x, t32}, mk(p.ws, WS_END), n < 2 ? cp.yo + n * 1024 : o2);
}

#define V10(a) "v" #a "0", "v" #a "1", "v" #a "2", "v" #a "3", "v" #a "4", "v" #a "5", "v" #a "6", "v" #a "7", "v" #a "8", "v" #a "9"
__device__ __forceinline__ int xs_at(int a, int lane) { return (lane & 15) * 128 + (((2 * a + (lane >> 5)) ^ (lane & 7)) << 4) + ((lane >> 4) & 1) * 8; }
#define F_NAP 1
#define YAT "1"
#define F_PRIO 1
__device__ __forceinline__ void finish(const Params& p, LAS char* L, const v2u (&xv)[NT], int t0, int rng, int G2, int a, unsigned t32, int lane) {
  const int i16 = lane & 15, g = lane >> 4;
  f32x2 st[NT];
#pragma unroll
  for (int u = 0; u < NT; ++u) st[u] = *(LAS f32x2*)(L + L_STAT2 + (16 * u + i16) * 8);
  const v2u bv = *(LAS v2u*)(L + L_B2 + a * 32 + g * 8);
  SB();
  f32x32 x, s;
#pragma unroll
  for (int u = 0; u < NT; ++u) {
    x[4 * u] = blo(xv[u].x), x[4 * u + 1] = bhi(xv[u].x), x[4 * u + 2] = blo(xv[u].y), x[4 * u + 3] = bhi(xv[u].y);
#pragma unroll
    for (int r = 0; r < 4; ++r) {
      float d = x[4 * u + r] - st[u].x;
      asm("" : "+v"(d));
      d *= st[u].y;
      asm("" : "+v"(d));
      s[4 * u + r] = d;
    }
  }
  x[24] = blo(bv.x), x[25] = bhi(bv.x), x[26] = blo(bv.y), x[27] = bhi(bv.y);
  x[28] = x[30] = -1.442695041f, x[29] = x[31] = 1.f;
  const int zb = (int)(size_t)L + a * 1024, yb = WS_YT + (64 * G2 + 16 * a) * YS + rng * 256;
  s[24] = __int_as_float(zb + lane * 16), s[25] = s[26] = __int_as_float(zb + 65536 + lane * 16);
  s[27] = __int_as_float((int)(size_t)L + L_FLAG + (F_DONE + (lane & 3)) * 4);
  s[28] = __int_as_float(4 * g * YS + i16 * 16);
  s[29] = s[30] = s[31] = __int_as_float((16 * t0 + i16) * 1280 + G2 * 128 + a * 32 + g * 8);
  int cnt, tmp;
  asm volatile(
      ".macro OP8 op, d, a, b, da, db\n"
      " .set e, 0\n .rept 8\n \\op v[\\d+e], v[\\a+(e&3)+\\da*(e/4)], v[\\b+(e&3)+\\db*(e/4)]\n .set e, e+1\n .endr\n"
      ".endm\n"
      ".macro PK4 op, d, a, b, da, db\n"
      " .set e, 0\n .rept 4\n .set a, \\a+(e&2)+\\da*(e/4)\n .set b, \\b+(e&2)+\\db*(e/4)\n"
      "  \\op v[\\d+e:\\d+e+1], v[a:a+1], v[b:b+1]\n .set e, e+2\n .endr\n"
      ".endm\n"
      ".macro TR8 op\n"
      " .set e, 0\n .rept 8\n \\op v[120+e], v[120+e]\n .set e, e+1\n .endr\n"
      ".endm\n"
      ".macro YLD\n"
      " buffer_load_dwordx4 v[96:99], v60, %4, %6 offen sc1\n buffer_load_dwordx4 v[100:103], v60, %4, %7 offen sc1\n"
      " buffer_load_dwordx4 v[104:107], v60, %4, %8 offen sc1\n buffer_load_dwordx4 v[108:111], v60, %4, %9 offen sc1\n"
      ".endm\n"
      ".macro PUT u, o\n"
      " v_cvt_pk_bf16_f32 v[\\o], v[\\o], v[\\o+1]\n v_cvt_pk_bf16_f32 v[\\o+1], v[\\o+2], v[\\o+3]\n"
      " s_mov_b32 %3, \\u*16*1280\n"
      " buffer_store_dwordx2 v[\\o:\\o+1], v61, %5, %3 offen\n"
      ".endm\n"
      ".macro ZDT P\n"
      " .set n, 0\n"
      " .rept 8\n"
      "  .set t, 2*\\P+((n/4)^(n&1))\n"
      "  ds_read_b128 v[64+4*n:67+4*n], v[56+t/4] offset:(t&3)*16384+(n&3)*4096\n"
      "  .set n, n+1\n"
      " .endr\n"
      ".endm\n"
      ".macro ZRD P\n"
      " s_mov_b32 %2, 0x400000\n"
      " 8:\n"
      " ds_read_b32 v128, v59 offset:16*\\P\n"
      " s_sub_u32 %2, %2, 1\n"
      " s_cbranch_scc1 9f\n"
      " .if \\P == 0\n s_sleep 1\n .endif\n"
      " s_waitcnt lgkmcnt(0)\n"
      " v_cmp_eq_u32 vcc, 0, v128\n"
      " s_cbranch_vccnz 8b\n"
      " 9:\n"
      " ZDT \\P\n"
      ".endm\n"
      "s_branch 4f\n"
      "1:\n"
      "YLD\n"
      "s_branch 2f\n"
      "4:\n"
      ".set P, 0\n"
      ".rept 2\n"
      " ZRD P\n"
      " .if P == 0\n"  " .endif\n"
      " .if (P == 0) && (" YAT " == 0)\n YLD\n .endif\n"
      " s_waitcnt lgkmcnt(0)\n"
      " OP8 v_add_f32, 112, 64, 68, 16, 16\n"
      " OP8 v_add_f32, 120, 72, 76, 16, 16\n"
      " OP8 v_add_f32, 112, 112, 24, 4, 0\n"
      " OP8 v_add_f32, 112, 112, 120, 4, 4\n"
      " .set e, 0\n .rept 8\n v_mul_f32 v[120+e], v28, v[112+e]\n .set e, e+1\n .endr\n"
      " TR8 v_exp_f32\n"
      " .if (P == 0) && (" YAT " == 1)\n YLD\n .endif\n"
      " OP8 v_mul_f32, 32+8*P, 32+8*P, 112, 4, 4\n"
      " .set e, 0\n .rept 8\n v_add_f32 v[120+e], 1.0, v[120+e]\n .set e, e+1\n .endr\n"
      " TR8 v_rcp_f32\n"
      " s_nop 0\n"
      " OP8 v_mul_f32, 32+8*P, 32+8*P, 120, 4, 4\n"
      " .if (P == 0) && (" YAT " == 2)\n YLD\n .endif\n"
      " .set P, P+1\n"
      ".endr\n"
      "s_mov_b32 %2, 0x8000\n"
      "2:\n"
      "s_waitcnt vmcnt(0)\n"
      "v_xor_b32 v112, %10, v99\n v_xor_b32 v113, %10, v103\n v_xor_b32 v114, %10, v107\n v_xor_b32 v115, %10, v111\n"
      "v_or3_b32 v112, v112, v113, v114\n v_or_b32 v112, v112, v115\n"
      "s_sub_u32 %2, %2, 1\n"
      "v_cmp_ne_u32 vcc, 0, v112\n"
      "s_cbranch_scc1 3f\n"
      "s_cbranch_vccnz 1b\n"
      "3:\n"
      "ds_read_b32 v128, v59 offset:32\n"
      "ZDT 2\n"
      ".set u, 5\n"
      ".rept 6\n"
      " .set o, 112+4*(u&3)\n"
      " .set r, 0\n"
      " .rept 4\n"
      "  .if u&1\n v_and_b32 v[o+r], 0xffff0000, v[96+4*r+u/2]\n .else\n v_lshlrev_b32 v[o+r], 16, v[96+4*r+u/2]\n .endif\n"
      "  .set r, r+1\n"
      " .endr\n"
      " .set r, 0\n"
      " .rept 2\n"
      "  .set c, 4*u+r\n"
      "  .if u < 4\n v_pk_fma_f32 v[o+r:o+r+1], v[o+r:o+r+1], v[32+c:33+c], v[c:c+1]\n .else\n v_pk_mul_f32 v[32+c:33+c], v[o+r:o+r+1], v[32+c:33+c]\n .endif\n"
      "  .set r, r+2\n"
      " .endr\n"
      " .if u < 4\n PUT u, o\n .endif\n"
      " .set u, u-1\n"
      ".endr\n"
      "s_waitcnt lgkmcnt(0)\n"
      "v_cmp_eq_u32 vcc, 0, v128\n"
      "s_cbranch_vccz 5f\n"
      "ZRD 2\n"
      "s_waitcnt lgkmcnt(0)\n"
      "5:\n"
      "PK4 v_pk_add_f32, 112, 64, 68, 16, 16\n"
      "PK4 v_pk_add_f32, 120, 72, 76, 16, 16\n"
      "PK4 v_pk_add_f32, 112, 112, 24, 4, 0\n"
      "PK4 v_pk_add_f32, 112, 112, 120, 4, 4\n"
      ".set e, 0\n .rept 4\n v_pk_mul_f32 v[120+e:121+e], v[112+e:113+e], v[28:29] op_sel_hi:[1,0]\n .set e, e+2\n .endr\n"
      "TR8 v_exp_f32\n"
      "PK4 v_pk_mul_f32, 48, 48, 112, 4, 4\n"
      ".set e, 0\n .rept 4\n v_pk_add_f32 v[120+e:121+e], v[120+e:121+e], v[28:29] op_sel:[0,1]\n .set e, e+2\n .endr\n"
      "TR8 v_rcp_f32\n"
      "s_nop 0\n"
      ".set e, 0\n .rept 4\n v_pk_fma_f32 v[112+e:113+e], v[120+e:121+e], v[48+e:49+e], v[16+e:17+e]\n .set e, e+2\n .endr\n"
      "PUT 4, 112\n"
      "PUT 5, 116\n"
      ".purgem OP8\n .purgem PK4\n .purgem TR8\n .purgem YLD\n .purgem ZDT\n .purgem PUT\n .purgem ZRD\n"
      : "+{v[0:31]}"(x), "+{v[32:63]}"(s), "=&s"(cnt), "=&s"(tmp)
      : "s"(mkd(p.ws, WS_END)), "s"(mkd(p.out, S * D * 2)), "s"(yb), "s"(yb + YS), "s"(yb + 2 * YS), "s"(yb + 3 * YS), "s"(t32)
      : "memory", "scc", "vcc", "v64", "v65", "v66", "v67", "v68", "v69", V10(7), V10(8), V10(9), V10(10), V10(11), V10(12), V10(13)
  );
}

#define WRN "2"
#ifndef M_PRIO
#define M_PRIO "2"
#endif
#define WB "2"
#define B9 "2"
#define HB "7"
#define HN "31"
__device__ __forceinline__ void mma_waves(const Params& p, LAS char* L, int i, int j, bool job, int t0, int G2, int rot, int w, int lane) {
  const int fh = w & 1, th = w >> 1, i16 = lane & 15, g = lane >> 4, q = (lane >> 2) & 3, Lb = (int)(size_t)L;
  const int fb = 128 * j + 64 * fh;
  v2u b1v[4];
#pragma unroll
  for (int m = 0; m < 4; ++m) b1v[m] = __builtin_amdgcn_raw_buffer_load_b64(mk(p.b1, F * 2), (fb + 16 * m + 4 * g) * 2, 0, 0);
  const v2u b2v = __builtin_amdgcn_raw_buffer_load_b64(mk(p.b2, D * 2), (64 * G2 + 16 * w + 4 * g) * 2, 0, 0);
  SB();
  const int sb = 27 * th + 13 * fh, ss = 26 + 27 * th;
  i32x16 va, vb;
#pragma unroll
  for (int m = 0; m < 4; ++m) va[m] = Lb + L_W1 + (8 * g + q) * 256 + (((4 * fh + m) ^ (q | ((g & 1) << 2))) << 5) + 8 * (lane & 3);
#pragma unroll
  for (int ks = 0; ks < 2; ++ks) va[4 + ks] = Lb + (64 * th + i16) * 128 + (((4 * ks + g) ^ (i16 >> 1)) << 4);
  va[6] = Lb + L_STAT + (64 * th + i16) * 8;
  va[7] = Lb + L_CS + (64 * fh + 4 * g) * 4;
  va[8] = Lb + L_FLAG + w * 4;
  va[9] = Lb + L_FLAG + (lane & 3) * 4;
  va[10] = lane * 16 + WS_HT + (4 * j + 2 * fh) * HS + (8 * i + 4 * th) * 1024;
  va[11] = va[10] + HS;
  va[14] = 1;
  {
    int so[4];
    w2_src(so, lane);
    va[15] = so[0];
  }
  {
    const int rr = 4 * g + q;
#pragma unroll
    for (int dd = 0; dd < 4; ++dd) {
      vb[dd] = Lb + w * 4096 + rr * 128 + ((dd ^ ((rr >> 1) & 3)) << 5) + 8 * (lane & 3);
      vb[4 + dd] = vb[dd] + 65536;
    }
    vb[8] = lane * 16 + fh * 1024;
    vb[9] = lane * 16 + 1024 - fh * 1024;
    vb[10] = vb[8] + 4096;
    vb[11] = vb[9] + 4096;
    vb[12] = Lb + w * 4096 + lane * 16;
    vb[13] = vb[12] + 65536;
    vb[14] = vb[15] = va[8];
    va[12] = lane == 0 ? flag_at(FH + (2 * i + th) * 32 + 2 * j + fh) : OOB;
    const int tl = min(t0 + 4 * (lane >> 3), t0 + NT - 1), c = lane & 7;
    va[13] = flag_at(FH + (tl >> 2) * 32 + (c == 7 ? ss >> 1 : (sb >> 1) + c));
  }
  f32x32 acc[2];
  asm volatile(
      ".set n, 0\n"
      ".rept 32\n"
      " v_mov_b64 v[n:n+1], 0\n"
      " .set n, n+2\n"
      ".endr\n"
      : "={v[0:31]}"(acc[0]), "={v[32:63]}"(acc[1]), "+{v[128:143]}"(va));
  i32x16 sh;
  i32x8 sw;
#pragma unroll
  for (int k = 0; k < 16; ++k)
    sh[k] = WS_HT + t0 * 1024 + g2_step(w, rot, min(k, 13)) * HS;
#pragma unroll
  for (int k = 0; k < 8; ++k) sw[k] = G2 * 128 + g2_step(w, rot, 5 + k) * 40960;
  int role = __builtin_amdgcn_readfirstlane((fb < F ? 1 : 0) + (job ? 2 : 0));
  asm volatile("" : "+{s[52:67]}"(sh), "+{s[68:75]}"(sw), "+s"(role));
  KvPre kp;
  kv_pre(kp, L + L_KV, L + L_KT, w, lane);
  BAR();
  kv_make<1>(kp);
  SB();
  f32x16 bb;
#pragma unroll
  for (int m = 0; m < 4; ++m) {
    bb[4 * m] = blo(b1v[m].x);
    bb[4 * m + 1] = bhi(b1v[m].x);
    bb[4 * m + 2] = blo(b1v[m].y);
    bb[4 * m + 3] = bhi(b1v[m].y);
  }
  *(LAS v2u*)(L + L_B2 + w * 32 + g * 8) = b2v;
  {
    int cnt, zo = 65536 - Lb - w * 4096;
    asm volatile(
        ".macro G1RD n, s, sl, ks\n"
        " .if (\\n %% 3) == 2\n"
        "  ds_read_b128 v[\\s+16+4*(\\n/3):\\s+19+4*(\\n/3)], v[132+\\ks] offset:\\sl*16384+(\\n/3)*2048\n"
        " .else\n"
        "  ds_read_b64_tr_b16 v[\\s+4*(\\n/3)+2*(\\n%%3):\\s+4*(\\n/3)+2*(\\n%%3)+1], v[128+\\n/3] offset:\\sl*16384+\\ks*8192+(\\n%%3)*1024\n"
        " .endif\n"
        ".endm\n"
        ".macro STRD n\n"
        " .if \\n == 0\n"
        "  ds_read_b32 v164, v137 offset:%c12*4\n"
        " .elseif \\n < 5\n"
        "  ds_read_b64 v[166+2*\\n:167+2*\\n], v134 offset:(\\n-1)*128\n"
        " .else\n"
        "  ds_read_b128 v[156+4*\\n:159+4*\\n], v135 offset:(\\n-5)*64\n"
        " .endif\n"
        ".endm\n"
        ".macro G1H c, s, sl, ks, st\n"
        " .set n, 0\n"
        " .rept 16\n"
        "  v_mfma_f32_16x16x32_bf16 v[4*n:4*n+3], v[\\c+4*(n&3):\\c+4*(n&3)+3], v[\\c+16+4*(n/4):\\c+19+4*(n/4)], v[4*n:4*n+3]\n"
        "  .if (n < 12) && (\\sl >= 0)\n"
        "   G1RD n, \\s, \\sl, \\ks\n"
        "  .endif\n"
        "  .if (n >= 4) && (n < 13) && \\st\n"
        "   .set m, n-4\n"
        "   STRD m\n"
        "  .endif\n"
        "  .set n, n+1\n"
        " .endr\n"
        ".endm\n"
        ".macro LWAIT a, f, v\n"
        " s_mov_b32 %7, 0x400000\n"
        " 8:\n"
        " ds_read_b32 v255, \\a offset:\\f*4\n"
        " s_sub_u32 %7, %7, 1\n"
        " s_cbranch_scc1 9f\n"
        " s_waitcnt lgkmcnt(0)\n"
        " v_cmp_gt_i32 vcc, \\v, v255\n"
        " s_cbranch_vccnz 8b\n"
        " 9:\n"
        ".endm\n"
        ".macro PK4 op, d, a, b, c, da, db, dc, mod:vararg\n"
        " .set e, 0\n"
        " .rept 4\n"
        "  .if \\c >= 0\n"
        "   \\op v[\\d+e:\\d+e+1], v[\\a+\\da*e:\\a+\\da*e+1], v[\\b+\\db*e:\\b+\\db*e+1], v[\\c+\\dc*e:\\c+\\dc*e+1] \\mod\n"
        "  .else\n"
        "   \\op v[\\d+e:\\d+e+1], v[\\a+\\da*e:\\a+\\da*e+1], v[\\b+\\db*e:\\b+\\db*e+1] \\mod\n"
        "  .endif\n"
        "  .set e, e+2\n"
        " .endr\n"
        ".endm\n"
        ".macro TR8 op\n"
        " .set e, 0\n"
        " .rept 8\n"
        "  \\op v[64+e], v[64+e]\n"
        "  .set e, e+1\n"
        " .endr\n"
        ".endm\n"
        ".macro HST y\n"
        " buffer_store_dwordx4 v[16*(\\y&3)+8*(\\y/4):16*(\\y&3)+8*(\\y/4)+3], v[138+\\y/4], %8, 0 offen offset:(\\y&3)*1024 sc1\n"
        ".endm\n"
        ".macro HSL n\n"
        " .if (y >= " HB ") && (yp < y) && (" HN " & \\n)\n"
        "  HST yp\n"
        "  .set yp, yp+1\n"
        " .endif\n"
        ".endm\n"
        ".macro PUBLISH n\n"
        " s_waitcnt vmcnt(\\n)\n"
        " buffer_store_dwordx2 v[68:69], v140, %8, 0 offen sc1\n"
        ".endm\n"
        ".macro G2RD k, n\n"
        " .set b, (0x63052417 >> (4*(\\k&7))) & 7\n"
        " .set ws, 144+16*(\\k%%6)\n"
        " ds_read_b64_tr_b16 v[ws+2*\\n:ws+2*\\n+1], v[240+4*(b/4)+\\n/2] offset:(b&3)*16384+(\\n&1)*2048\n"
        ".endm\n"
        ".macro G2RDS q0, cnt\n"
        " .set rq, \\q0\n"
        " .rept \\cnt\n"
        "  .set rk, rq/8\n"
        "  .set rn, rq&7\n"
        "  G2RD rk, rn\n"
        "  .set rq, rq+1\n"
        " .endr\n"
        ".endm\n"
        ".macro W2LD k\n"
        " s_add_u32 m0, %19, ((0x63052417 >> (4*(\\k&7))) & 7)*16384\n"
        " s_nop 0\n"
        " buffer_load_dwordx4 v143, %17, s[63+\\k] offen lds\n"
        " buffer_load_dwordx4 v72, %17, s[63+\\k] offen offset:1024 lds\n"
        " buffer_load_dwordx4 v73, %17, s[63+\\k] offen offset:2048 lds\n"
        " buffer_load_dwordx4 v74, %17, s[63+\\k] offen offset:3072 lds\n"
        ".endm\n"
        ".macro G2AT q\n"
        " .if \\q < 48\n"
        "  .set gk, \\q / 6\n"
        "  .set gu, \\q %% 6\n"
        " .else\n"
        "  .set gu, 2*((\\q-48)/11)\n"
        "  .set gk, 8+(\\q-48)%%11\n"
        "  .if gk > 12\n"
        "   .set gu, gu+1\n"
        "   .set gk, gk-5\n"
        "  .endif\n"
        " .endif\n"
        ".endm\n"
        ".macro G2LD q\n"
        " G2AT \\q\n"
        " buffer_load_dwordx4 v[96+4*(\\q%%12):99+4*(\\q%%12)], v[248+(gu&1)+2*(gu/4)], %8, s[52+gk] offen offset:(gu&2)*1024 sc1\n"
        ".endm\n"
        ".macro G2MF d\n"
        " .set ws, 144+16*(k%%6)+4*\\d\n"
        " .if k > 0\n"
        "  v_mfma_f32_16x16x32_bf16 v[16*u+4*\\d:16*u+4*\\d+3], v[ws:ws+3], v[96+4*(q%%12):99+4*(q%%12)], v[16*u+4*\\d:16*u+4*\\d+3]\n"
        " .else\n"
        "  v_mfma_f32_16x16x32_bf16 v[16*u+4*\\d:16*u+4*\\d+3], v[ws:ws+3], v[96+4*q:99+4*q], 0\n"
        " .endif\n"
        ".endm\n"
        ".macro G2WR n\n"
        " .set dw, " WRN "*(k-8)+\\n/2-2+" WRN "\n"
        " .if (k >= 8) && (u > 0) && (\\n&1) && (dw >= " WRN "*(k-8)) && (dw < 4)\n"
        "  .set t, u-1\n"
        "  ds_write_b128 v[252+t/4], v[16*t+4*dw:16*t+4*dw+3] offset:(t&3)*16384+dw*1024\n"
        "  .if (t&1) && (dw == 3)\n"
        "   ds_write_b32 v254, v254 offset:(%c14+4*(t/2))*4\n"
        "  .endif\n"
        " .endif\n"
        ".endm\n"
        "s_branch 20f\n"
        "3:\n"
        "LWAIT v137, %c12, 1\n"
        ".set m, 1\n"
        ".rept 8\n"
        " STRD m\n"
        " .set m, m+1\n"
        ".endr\n"
        "s_waitcnt lgkmcnt(0)\n"
        "s_branch 4f\n"
        "5:\n"
        "s_barrier\n"
        "s_branch 6f\n"
        "10:\n"
        "PUBLISH 0\n"
        "s_branch 7f\n"
        "11:\n"
        "LWAIT v254, 4, 7\n"
        "s_branch 12f\n"
        "20:\n"
        "v_mov_b32 v160, 0xbdd2d3e8\n"
        "v_mov_b32 v161, 0xc0135761\n"
        "v_mov_b32 v162, 1.0\n"
        "v_and_b32 v163, -16, v136\n"
        "s_mov_b32 %7, 2\n"
        "s_waitcnt lgkmcnt(0)\n"
        "s_barrier\n"
        "ds_read_b64 v[166:167], v163 offset:%c10\n"
        ".set n, 0\n"
        ".rept 12\n"
        " G1RD n, 64, 0, 0\n"
        " .set n, n+1\n"
        ".endr\n"
        "1:\n"
        "s_waitcnt lgkmcnt(0)\n"
        "G1H 64, 96, 0, 1, 0\n"
        "s_waitcnt lgkmcnt(0)\n"
        "s_barrier\n"
        "G1H 96, 64, 1, 0, 0\n"
        "s_waitcnt lgkmcnt(0)\n"
        "G1H 64, 96, 1, 1, 0\n"
        "s_waitcnt lgkmcnt(0)\n"
        "s_barrier\n"
        "G1H 96, 64, 2, 0, 0\n"
        "s_waitcnt lgkmcnt(0)\n"
        "G1H 64, 96, 2, 1, 0\n"
        "s_waitcnt lgkmcnt(0)\n"
        "s_barrier\n"
        "s_sub_u32 %7, %7, 1\n"
        "G1H 96, 64, 0, 0, 0\n"
        "s_cbranch_scc0 1b\n"
        "s_waitcnt lgkmcnt(0)\n"
        "G1H 64, 96, 0, 1, 1\n"
        "s_waitcnt lgkmcnt(0)\n"
        "v_cmp_eq_u32 vcc, 0, v164\n"
        "s_bitcmp1_b32 %11, 0\n"
        "G1H 96, 64, -1, 0, 0\n"
        "s_setprio 2\n"
        "s_cbranch_scc0 5b\n"
        "s_cbranch_vccnz 3b\n"
        "4:\n"
        ".set y, 0\n"
        ".set yp, 0\n"
        ".rept 8\n"
        " .set a, 16*(y&3)+8*(y/4)\n"
        " .set t, 168+2*(y&3)\n"
        " PK4 v_pk_fma_f32, a, t, 176+8*(y/4), a, 0, 1, 1, op_sel_hi:[0,1,1] neg_lo:[1,0,0] neg_hi:[1,0,0]\n"
        " HSL 1\n"
        " PK4 v_pk_fma_f32, a, a, t, 144+8*(y/4), 1, 0, 1, op_sel:[0,1,0]\n"
        " .if y == " B9 "\n"
        "  s_barrier\n"
        " .endif\n"
        " PK4 v_pk_mul_f32, 64, a, a, -1, 1, 1, 0\n"
        " PK4 v_pk_fma_f32, 64, 64, 160, 160, 1, 0, 0, op_sel:[0,0,1] op_sel_hi:[1,0,1]\n"
        " PK4 v_pk_mul_f32, 64, a, 64, -1, 1, 1, 0\n"
        " HSL 2\n"
        " TR8 v_exp_f32\n"
        " HSL 4\n"
        " PK4 v_pk_add_f32, 64, 64, 162, -1, 1, 0, 0, op_sel_hi:[1,0]\n"
        " HSL 8\n"
        " TR8 v_rcp_f32\n"
        " HSL 16\n"
        " PK4 v_pk_mul_f32, a, a, 64, -1, 1, 1, 0\n"
        " v_cvt_pk_bf16_f32 v[a], v[a], v[a+1]\n"
        " v_cvt_pk_bf16_f32 v[a+1], v[a+2], v[a+3]\n"
        " v_cvt_pk_bf16_f32 v[a+2], v[a+4], v[a+5]\n"
        " v_cvt_pk_bf16_f32 v[a+3], v[a+6], v[a+7]\n"
        " .set y, y+1\n"
        ".endr\n"
        ".rept 8-yp\n"
        " HST yp\n"
        " .set yp, yp+1\n"
        ".endr\n"
        "6:\n"
        "v_mov_b64 v[68:69], v[166:167]\n"
        "s_bitcmp1_b32 %11, 1\n"
        "v_add_u32 v72, 9216, v143\n"
        "v_add_u32 v73, 2*9216, v143\n"
        "v_add_u32 v74, 3*9216, v143\n"
        "s_barrier\n"
        "s_cbranch_scc0 10b\n"
        ".set k, 5\n"
        ".rept " WB "\n"
        " W2LD k\n"
        " .set k, k+1\n"
        ".endr\n"
        "G2RDS 0, 40\n"
        "PUBLISH 4*" WB "\n"
        ".rept 3-" WB "\n"
        " W2LD k\n"
        " .set k, k+1\n"
        ".endr\n"
        "s_waitcnt lgkmcnt(0)\n"
        "W2LD 8\n"
        "W2LD 9\n"
        "W2LD 10\n"
        "W2LD 11\n"
        "s_mov_b64 exec, 0xffff\n"
        "s_mov_b32 %7, 0x8000\n"
        "8:\n"
        "buffer_load_dwordx2 v[70:71], v141, %8, 0 offen sc1\n"
        "s_sub_u32 %7, %7, 1\n"
        "s_cbranch_scc1 9f\n"
        "s_waitcnt vmcnt(0)\n"
        "v_cmp_ne_u64 vcc, v[68:69], v[70:71]\n"
        "s_mov_b64 exec, vcc\n"
        "s_cbranch_vccnz 8b\n"
        "9:\n"
        "s_mov_b64 exec, -1\n"
        ".set q, 0\n"
        ".rept 11\n"
        " G2LD q\n"
        " .set q, q+1\n"
        ".endr\n"
        "s_setprio 0\n"
        ".set q, 0\n"
        ".rept 81\n"
        " G2AT q\n"
        " .set k, gk\n"
        " .set u, gu\n"
        " .set o, 10\n"
        " .if q > 70\n"
        "  .set o, 80-q\n"
        " .endif\n"
        " .if ((q%%6) == 0) && (q >= 6) && (q <= 54)\n"
        "  s_waitcnt vmcnt(o) lgkmcnt(0)\n"
        " .else\n"
        "  s_waitcnt vmcnt(o)\n"
        " .endif\n"
        " G2MF 0\n"
        " .set ql, q+11\n"
        " .if ql < 81\n"
        "  G2LD ql\n"
        " .endif\n"
        " G2WR 0\n"
        " G2MF 1\n"
        " .if q == 6\n"
        "  ds_write_b32 v254, v254 offset:%c13*4\n"
        " .endif\n"
        " .if q == 41\n"
        "  ds_read_b32 v255, v254 offset:16\n"
        " .endif\n"
        " .if q == 42\n"
        "  v_cmp_gt_i32 vcc, 7, v255\n"
        " .endif\n"
        " G2WR 1\n"
        " G2MF 2\n"
        " .set kr, q/6+5\n"
        " .set ur, q%%6\n"
        " .if (ur >= 1) && (ur <= 4) && (q < 54)\n"
        "  .set n, 2*ur-2\n"
        "  G2RD kr, n\n"
        " .endif\n"
        " G2WR 2\n"
        " G2MF 3\n"
        " .if (ur >= 1) && (ur <= 4) && (q < 54)\n"
        "  .set n, 2*ur-1\n"
        "  G2RD kr, n\n"
        " .endif\n"
        " G2WR 3\n"
        " .if q == 42\n"
        "  s_cbranch_vccnz 11b\n"
        "  12:\n"
        "  s_setprio " M_PRIO "\n"
        " .endif\n"
        " .set q, q+1\n"
        ".endr\n"
        "s_nop 4\n"
        ".set n, 0\n .rept 4\n ds_write_b128 v253, v[80+4*n:83+4*n] offset:16384+n*1024\n .set n, n+1\n .endr\n"
        "ds_write_b32 v254, v254 offset:(%c14+8)*4\n"
        "7:\n"
        ".purgem G1RD\n .purgem STRD\n .purgem G1H\n .purgem LWAIT\n .purgem PK4\n .purgem TR8\n .purgem HST\n .purgem HSL\n .purgem PUBLISH\n"
        ".purgem G2RD\n .purgem G2RDS\n .purgem G2AT\n .purgem G2WR\n .purgem W2LD\n .purgem G2LD\n .purgem G2MF\n"
        : "+{v[0:31]}"(acc[0]), "+{v[32:63]}"(acc[1]), "+s"(zo), "+{v[128:143]}"(va), "+{v[144:159]}"(bb), "+{v[240:255]}"(vb), "+{s[52:67]}"(sh), "=&s"(cnt)
        : "s"(mkd(p.ws, WS_END)), "n"(0), "n"(L_TAG - L_FLAG), "s"(role), "n"(F_STAT), "n"(F_GELU), "n"(F_DONE), "n"(0), "n"(HS),
          "s"(mkd(p.w2, F * D * 2)), "{s[68:75]}"(sw), "s"(Lb + w * 4096)
        : "memory", "scc", "vcc", "v64", "v65", "v66", "v67", "v68", "v69", V10(7), V10(8), V10(9), V10(10), V10(11), "v120", "v121", "v122", "v123", "v124", "v125", "v126", "v127",
          V10(16), V10(17), V10(18), V10(19), V10(20), V10(21), V10(22), V10(23)
    );
  }
}

constexpr int P_KT = 6 * CONV_BUF, P_KV = P_KT + 24576;
static_assert(P_KV + 3 * KV_IN <= L_FLAG, "lds");
__device__ __forceinline__ float dot2(unsigned a, unsigned b, float c) {
  return __builtin_amdgcn_fdot2_f32_bf16(__builtin_bit_cast(bf2_t, a), __builtin_bit_cast(bf2_t, b), c, false);
}
__device__ __forceinline__ float sum8(float v) {
  v += __int_as_float(__builtin_amdgcn_mov_dpp(__float_as_int(v), 0xb1, 0xf, 0xf, true));
  v += __int_as_float(__builtin_amdgcn_mov_dpp(__float_as_int(v), 0x4e, 0xf, 0xf, true));
  return v + __int_as_float(__builtin_amdgcn_mov_dpp(__float_as_int(v), 0x141, 0xf, 0xf, true));
}
__device__ __forceinline__ void prep_load(const Params& p, LAS char* L, int pp, int w, int lane) {
  const int row = lane >> 3, o = (64 * pp + 32 * (w & 1) + row) * 1280 + 7 * 128 + (((lane & 7) ^ ((row >> 1) & 2)) << 4);
  const int vo[4] = {o ^ 64, o + 9216, (o ^ 64) + 2 * 9216, o + 3 * 9216};
  const v4u rk = mkd(w >= 2 ? p.v : p.k, S * D * 2);
#pragma unroll
  for (int n = 0; n < 3; ++n) {
    if (n == 2) dma4(rk, L + P_KV + n * KV_IN + 1024 + w * 4096, vo, n * 128);
    if (w == 0) dma16(rk, L + P_KV + n * KV_IN, row >= 5 ? o - 8 * 1280 : OOB, n * 128);
    if (w == 3) dma16(mkd(p.sk, D * 8), L + P_KV + n * KV_IN + 17408, lane < 32 ? lane * 16 : OOB, (7 + n) * 512);
  }
  __builtin_amdgcn_s_barrier();
}
__device__ __forceinline__ unsigned prep_kv(const Params& p, LAS char* L, int pp, int w, int lane) {
  const int c0 = 448 + 6 * pp;
  u64 ep = ask_epoch(p);
  KvPre kp;
  kv_pre(kp, L + P_KV, L + P_KT, w, lane);
  asm volatile("s_waitcnt vmcnt(0) lgkmcnt(0)" ::: "memory");
  lds_post(L, F_GELU + w, 1);
  lds_wait4(L, F_GELU, lane);
  asm volatile("" : "+s"(ep));
  const u64 tag = tag_of(ep);
  kv_make<3>(kp);
#pragma unroll
  for (int n = 0; n < 3; ++n) kv_store<2>(p, L + P_KT + n * 8192, 2 * w, pp, 7 + n, lane);
  lds_post(L, F_RING + w, 1);
  lds_wait4(L, F_DONE, lane);
  WAIT_V(0);
  set_flag(p, lane < 3 ? flag_at(FK + (28 + 4 * lane + w) * 32 + pp) : OOB, tag);
#define P_NAP 20
  if (w == 3) {
    __builtin_amdgcn_s_sleep(P_NAP);
    wait_flag<2>(p, flag_at(FK + ((c0 + (lane < 32 ? 0 : 5)) >> 4) * 32 + (lane & 31)), tag);
    lds_post(L, F_STAT, 1);
  }
  lds_wait<1>(L, F_STAT, 1);
  stage_u(p, L + w * CONV_BUF, u_src(c0 + w, lane));
  if (w < 2) stage_u(p, L + (4 + w) * CONV_BUF, u_src(c0 + 4 + w, lane));
  WAIT_V(0);
  lds_post(L, F_STAGED + w, 1);
  return tag >> 32;
}
__device__ __forceinline__ unsigned prep_side(const Params& p, LAS char* L, int pp, int a, int lane) {
  const int c0 = 448 + 6 * pp + 3 * (a & 1);
  LAS char* Lc = L + 3 * (a & 1) * CONV_BUF;
  *(LAS int*)(L + L_FLAG + lane * 4) = 0;
  BAR();
  u64 ep, dd;
  v4u hh[3];
  asm volatile(
      "s_load_dwordx2 %0, %5, 0x0\n s_load_dwordx2 %1, %6, 0x0\n s_load_dwordx4 %2, %7, 0x0\n s_load_dwordx4 %3, %7, 0x1000\n"
      "s_load_dwordx4 %4, %7, 0x2000"
      : "=&s"(ep), "=&s"(dd), "=&s"(hh[0]), "=&s"(hh[1]), "=&s"(hh[2])
      : "s"(p.ws + WS_EPOCH), "s"(p.dv + (c0 & ~1) * 2), "s"(p.hk + c0 * 4096)
      : "memory");
  SB();
  const rsrc_t rx = mk(p.x, S * D * 2);
  const int row = lane >> 3, c = lane & 7;
  v4u xr[20];
#pragma unroll
  for (int n = 0; n < 20; ++n) xr[n] = __builtin_amdgcn_raw_buffer_load_b128(rx, (64 * pp + 16 * a + row) * 1280 + c * 16 + (n % 10) * 128, (n / 10) * 8 * 1280, 0);
  SB();
  asm volatile("s_waitcnt lgkmcnt(0)" : "+s"(ep), "+s"(dd), "+s"(hh[0]), "+s"(hh[1]), "+s"(hh[2])::"memory");
  lds_post(L, F_DONE + a, 1);
  const u64 tag = tag_of(ep);
  {
    float sx[2], sq[2];
#pragma unroll
    for (int h = 0; h < 2; ++h) {
      sx[h] = sq[h] = 0.f;
#pragma unroll
      for (int n = 0; n < 40; ++n) {
        const unsigned v = xr[10 * h + (n >> 2)][n & 3];
        sx[h] = dot2(v, 0x3f803f80u, sx[h]);
        sq[h] = dot2(v, v, sq[h]);
      }
      sx[h] = sum8(sx[h]);
      sq[h] = sum8(sq[h]);
    }
    const float mean = (c ? sx[1] : sx[0]) * (1.f / D);
    const float var = fmaxf((c ? sq[1] : sq[0]) * (1.f / D) - mean * mean, 0.f);
    const unsigned md = pk(mean, __builtin_amdgcn_sqrtf(rb(rb(var) + 1e-5f)));
    __builtin_amdgcn_raw_buffer_store_b64(v2u{md, (unsigned)(tag >> 32)}, mk(p.ws, WS_END), c < 2 ? WS_STAT + (64 * pp + 16 * a + 8 * c + row) * 8 : OOB, 0, SC1);
    SB();
  }
  if (a < 2) {
#pragma unroll
    for (int n = 0; n < 3; ++n) stage_pads(Lc + n * CONV_BUF, lane);
    dd >>= 16 * a;
#pragma unroll
    for (int c = 0; c < 3; ++c) stage_head(Lc + c * CONV_BUF, hh[c], ((unsigned)(dd >> (16 * c)) & 0xffffu) << 16);
  }
  lds_wait4<1>(L, F_RING, lane);
  if (a < 2) {
#pragma unroll
    for (int n = 0; n < 3; ++n) stage_h<0>(p, Lc + n * CONV_BUF, (c0 + n) * 4096 + lane * 16);
    WAIT_V(0);
#pragma unroll
    for (int n = 0; n < 3; ++n) stage_h<1>(p, Lc + n * CONV_BUF, (c0 + n) * 4096 + lane * 16);
    WAIT_V(0);
    lds_post(L, F_STAGED + 4 + a, 1);
  } else {
    touch(p.w2 + (448 * 32 + (2 * pp + a - 2) * 46 + min(lane, 45)) * 128, L + L_DUMP);
  }
  return tag >> 32;
}

__device__ __forceinline__ void next_epoch(const Params& p) {
  const u64 tag = get_tag(p);
  const int lane = lane_id();
#pragma unroll 1
  for (int spin = 0; spin < SPIN_CAP; ++spin) {
    bool ok = true;
#pragma unroll
    for (int n = 0; n < 4; ++n) {
      const v2u f = __builtin_amdgcn_raw_buffer_load_b64(mk(p.ws, WS_END), flag_at(FK + (lane >> 5) * 128 + (lane & 31)), n * 256 * FSTR, SC1);
      ok &= f.y == (unsigned)(tag >> 32) && f.x == (unsigned)tag;
    }
    if (__all(ok)) break;
    asm volatile("s_sleep 8" ::: "memory");
  }
  set_flag(p, lane == 0 ? WS_EPOCH : OOB, tag);
}

__global__ __launch_bounds__(512, 2) void h3_kernel(Params p) {
  extern __shared__ __attribute__((aligned(16))) char smem[];
  LAS char* L = (LAS char*)smem;
  int lane = threadIdx.x & 63;
  const int w = __builtin_amdgcn_readfirstlane(threadIdx.x >> 6);
  const unsigned bid = blockIdx.x;
  unsigned xcd = bid & 7, r = bid >> 3;
  v4u kvp;
  const bool big = bid < 224;
  const int who = big ? w & 6 : ~w & 4;
  asm volatile("s_cmp_eq_u32 %2, 4\n s_cbranch_scc0 1f\n s_load_dwordx4 %0, %1, 0x8\n 1:\n"
               : "={s[4:7]}"(kvp)
               : "s"(__builtin_amdgcn_kernarg_segment_ptr()), "s"(who)
               : "memory", "scc");
  SB();
  code_prefetch(__builtin_amdgcn_s_getpc() & ~255ull, L + L_DUMP, r + 32 * (w - 6), lane);
  SB();
  int um, uc, hs, ds, dm, sh, bo, vm;
  asm("s_cmpk_lt_u32 %8, 224\n s_cselect_b32 %0, 28, 4\n s_cselect_b32 %1, 0, 196\n s_cselect_b32 %2, 0, 32*1280\n s_cselect_b32 %3, 32*1280-128, 0\n"
      "s_cselect_b32 %4, 0, %9\n s_cselect_b32 %5, 13, 12\n s_cselect_b32 %6, 0, %10\n s_cselect_b32 %7, 1, 2"
      : "=s"(um), "=s"(uc), "=s"(hs), "=s"(ds), "=s"(dm), "=s"(sh), "=s"(bo), "=s"(vm)
      : "s"(bid), "n"(KV_IN - 4096), "n"(P_KV - L_KV + 32768)
      : "scc");
  const int jb = um * xcd + r + uc;
  const int ko = kv_head(kvp, who, (jb & 31) * (64 * 1280) + (jb >> 5) * 128 + (w & 1) * hs, ds + 128, (int)(size_t)L + L_KV + 1024 - 32768 + bo + (w << sh),
                         dm + 4096, w & vm, lane);
  SB();
  const int rr = (r * 37) >> 8, i = 4 * (xcd >> 1) + rr, j = 7 * (xcd & 1) + (r - 7 * rr);
  LoadPre lp;
  if (who == 4 && big) kv_rest(p, L, ko, jb, w & 1, lane);
  asm volatile("" : "+s"(xcd), "+s"(r));
  const bool g1 = r < 28;
  const int jx = 28 * (xcd >> 1) + r, rng = (jx * 205) >> 10, g5 = jx - 5 * rng, G2 = 5 * (xcd & 1) + g5, t0 = min(6 * rng, 122);
  const bool job = jx < 110;
  const int rot = (ROT >> (4 * g5)) & 15;
  if (__builtin_expect(g1 && w < 4, 0)) {
    mma_waves(p, L, i, j, job, t0, G2, rot, w, lane);
    if (bid == 223 && w == 0) next_epoch(p);
    __builtin_amdgcn_endpgm();
  }
  const int pp = 4 * xcd + (r - 28);
  __builtin_amdgcn_s_setprio(3);
  if (__builtin_expect(w < 4, 0)) prep_load(p, L, pp, w, lane);
  lane = lane_id();
  ConvPre cp;
  conv_pre(cp, L, L + (g1 ? L_CONV - 2 * CONV_BUF : 0) + (w >> 1) * CONV_BUF, (g1 ? 2 * jb - 2 : 448 + 6 * pp) + (w >> 1), w, lane);
  int so[4], xa = (int)(size_t)L + L_XS + xs_at(w & 3, lane);
  w2_src(so, lane);
  int s12 = g2_step(w & 3, rot, 12) * 40960 + G2 * 128, s13 = g2_step(w & 3, rot, 13) * 40960 + G2 * 128;
  asm volatile("" : "+v"(so[0]), "+v"(so[1]), "+v"(so[2]), "+v"(so[3]), "+v"(xa), "+s"(s12), "+s"(s13));
  unsigned t32;
  v2u xv[NT];
  u32x32 held;
  const v4u rw2 = mkd(p.w2, job ? F * D * 2 : 0);
  if (__builtin_expect(g1, 1)) {
    if (__builtin_expect(w < 6, 1)) gemm1_first(p, L, lp, ko, i, j, jb, w & 1, lane);
    if (__builtin_expect(w < 6, 1)) t32 = gemm1_load(p, L, lp, rw2, so, t0, G2, rot, jb, w - 4, lane);
    else t32 = gemm1_side(p, L, i, j, t0, jb, w - 6, lane);
    asm volatile(
        ".set n, 0\n .rept 4\n buffer_load_dwordx4 v[224+4*n:227+4*n], %1, %2, %3 offen\n buffer_load_dwordx4 v[240+4*n:243+4*n], %1, %2, %4 offen\n"
        " .if n < 3\n v_add_u32 %1, 8*1280, %1\n .endif\n .set n, n+1\n .endr\n"
        "s_setprio %5\n"
        : "=&{v[224:255]}"(held), "+v"(so[0])
        : "s"(rw2), "s"(s12), "s"(s13), "n"(H_PRIO)
        : "memory");
#pragma unroll
    for (int u = 0; u < NT; ++u) xv[u] = at<v2u>(xa + u * 2048);
  } else {
    if (w < 4) __builtin_amdgcn_s_setprio(2);
    if (w < 4) t32 = prep_kv(p, L, pp, w, lane);
    else t32 = prep_side(p, L, pp, w - 4, lane);
    lds_wait<1>(L, F_STAGED + min(lane & 7, 5), 1);
    asm volatile("" : "={v[224:255]}"(held));
  }
#pragma unroll 1
  for (int round = 0;; ++round) {
    conv(p, cp, w, round, g1, held, t32);
    if (__builtin_expect(w >= 4 || round, 1)) break;
    cp.ba += 4 * CONV_BUF;
    cp.aa += 4 * CONV_BUF, cp.lb += 4 * CONV_BUF, cp.cw += 4 * CONV_BUF, cp.kw += 4 * CONV_BUF, cp.yr += 4 * CONV_BUF;
    cp.yo += 4 * YS;
    asm volatile("" : "+v"(cp.ba), "+v"(cp.aa), "+v"(cp.lb), "+v"(cp.cw), "+v"(cp.kw), "+v"(cp.yr), "+v"(cp.yo));
  }
  if (g1 && job) {
    lane = lane_id();
    lds_wait_at<1, (F_GELU - 4) * 4>(cp.fl, 1);
#pragma unroll
    for (int n = 0; n < 8; ++n)
      *(LAS v4u*)(L + w2_block(12 + n / 4) * 16384 + (w - 4) * 4096 + (n & 3) * 1024 + lane * 16) = v4u{held[4 * n], held[4 * n + 1], held[4 * n + 2], held[4 * n + 3]};
    asm volatile("" ::: "memory");
    at<volatile int>(cp.fl) = 7;
    __builtin_amdgcn_s_setprio(F_PRIO);
    finish(p, L, xv, t0, rng, G2, w - 4, t32, lane);
  }
}
}

extern "C" void kernel_launch(void* const* d_in, const int* in_sizes, int n_in, void* d_out, int out_size, void* d_ws, size_t ws_size,
                              hipStream_t stream) {
  using namespace h3;
  Params p = {};
  p.x = (const char*)d_in[0];
  p.k = (const char*)d_in[1];
  p.v = (const char*)d_in[2];
  p.sk = (const char*)d_in[3];
  p.hk = (const char*)d_in[4];
  p.dv = (const char*)d_in[5];
  p.w1 = (const char*)d_in[7];
  p.b1 = (const char*)d_in[8];
  p.w2 = (const char*)d_in[9];
  p.b2 = (const char*)d_in[10];
  p.out = (char*)d_out;
  p.ws = (char*)d_ws;
  (void)hipFuncSetAttribute((const void*)h3_kernel, hipFuncAttributeMaxDynamicSharedMemorySize, LDS_BYTES);
  h3_kernel<<<256, 512, LDS_BYTES, stream>>>(p);
}
```
